```python
import torch

def _softmax(x, axis=-1):
    return torch.softmax(x, dim=axis)

def _gelu(x):
    return torch.nn.functional.gelu(x, approximate='tanh')

def _silu(x):
    return torch.nn.functional.silu(x)

def _sigmoid(x):
    return torch.sigmoid(x)

def _layernorm(x, axis=-1, eps=1e-05):
    m = x.mean(dim=axis, keepdim=True)
    v = x.var(dim=axis, keepdim=True, unbiased=False)
    return (x - m) / torch.sqrt(v + eps)

def _scatter_add(tgt, idx, src, axis=0):
    return tgt.clone().index_add_(axis, idx, src)

def _segment_sum(src, seg, n):
    out = torch.zeros((n,) + src.shape[1:], dtype=src.dtype)
    return out.index_add_(0, seg, src)

def _pad(x, pw):
    flat = tuple((v for pair in reversed(pw) for v in pair))
    return torch.nn.functional.pad(x, flat)
kernel_name = 'adaLN_cond__spectral_fnet__moe_arch_mod__prompt'

def setup_inputs():

    _w = lambda a: (lambda t: t.to(torch.bfloat16) if t.is_floating_point() else t)(torch.as_tensor(a))

    def _A_setup():
        B, S, D = (1, 2048, 448)
        ns = torch.arange(S)[:, None] * torch.arange(S)[None, :]
        nd = torch.arange(D)[:, None] * torch.arange(D)[None, :]
        Ws_re = torch.cos(-2.0 * torch.pi * ns / S).to(float)
        Ws_im = torch.sin(-2.0 * torch.pi * ns / S).to(float)
        Wd_re = torch.cos(-2.0 * torch.pi * nd / D).to(float)
        Wd_im = torch.sin(-2.0 * torch.pi * nd / D).to(float)
        return {'x': _w((lambda *s: torch.randn(*s, dtype=torch.bfloat16) * 0.05)(B, S, D)), 'dft_seq_re': _w(Ws_re), 'dft_seq_im': _w(Ws_im), 'dft_hid_re': _w(Wd_re), 'dft_hid_im': _w(Wd_im)}
    _A_in = _A_setup()

    def _B_setup():
        B, S, D, F, C = (1, 2048, 448, 1792, 1)
        return {'x': _w((lambda *s: torch.randn(*s, dtype=torch.bfloat16) * 0.05)(B, S, D)), 'w_r': _w((lambda *s: torch.randn(*s, dtype=torch.bfloat16) * 3.2)(D)), 'w_gate': _w((lambda *s: torch.randn(*s, dtype=torch.bfloat16) * 0.8)(D, F)), 'w_up': _w((lambda *s: torch.randn(*s, dtype=torch.bfloat16) * 0.8)(D, F)), 'w_down': _w((lambda *s: torch.randn(*s, dtype=torch.bfloat16) * 0.8)(F, D))}
    _B_in = _B_setup()
    return {'A_x': _A_in['x'], 'A_dft_seq_re': _A_in['dft_seq_re'], 'A_dft_seq_im': _A_in['dft_seq_im'], 'A_dft_hid_re': _A_in['dft_hid_re'], 'A_dft_hid_im': _A_in['dft_hid_im'], 'B_x': _B_in['x'], 'B_w_r': _B_in['w_r'], 'B_w_gate': _B_in['w_gate'], 'B_w_up': _B_in['w_up'], 'B_w_down': _B_in['w_down'], 'c': _w((lambda *s: torch.randn(*s, dtype=torch.bfloat16) * 0.1)(1, 448) * 8.0), 'w_mod': _w((lambda *s: torch.randn(*s, dtype=torch.bfloat16) * 0.1)(448, 6 * 448) * 448 ** (-0.5) * 8.0)}

def reference(A_x, A_dft_seq_re, A_dft_seq_im, A_dft_hid_re, A_dft_hid_im, B_x, B_w_r, B_w_gate, B_w_up, B_w_down, c, w_mod):

    def _A_ref(x, dft_seq_re, dft_seq_im, dft_hid_re, dft_hid_im):
        a = torch.einsum('bsd,ed->bse', x, dft_hid_re)
        b = torch.einsum('bsd,ed->bse', x, dft_hid_im)
        y = torch.einsum('ts,bse->bte', dft_seq_re, a) - torch.einsum('ts,bse->bte', dft_seq_im, b)
        return y

    def _B_ref(x, w_r, w_gate, w_up, w_down):
        B, S, D = x.shape
        C = 1
        r = x @ w_r
        idx = torch.argsort(-r, axis=-1)[:, :C]
        idx_e = (lambda x, n, axis: torch.repeat_interleave(x, n, dim=axis))(idx.reshape(B, C, 1), D, axis=2)
        x_sel = (lambda x, i, axis: torch.take_along_dim(x, i, dim=axis))(x, idx_e, axis=1)
        g = _silu(x_sel @ w_gate)
        u = x_sel @ w_up
        y = g * u @ w_down
        r_sel = _sigmoid((lambda x, i, axis: torch.take_along_dim(x, i, dim=axis))(r, idx, axis=1))
        y = y * r_sel.reshape(B, C, 1)
        b_off = (torch.arange(B) * S).reshape(B, 1)
        flat_idx = (idx + b_off).reshape(B * C)
        zero = (x * 0.0).reshape(B * S, D)
        upd = _scatter_add(zero, flat_idx, y.reshape(B * C, D), axis=0)
        return x + upd.reshape(B, S, D)
    x = A_x
    mod = (_silu(c) @ w_mod).reshape(c.shape[0], 6, 1, -1)
    sa, ca, ga, sb, cb, gb = (mod[:, 0].clone(), mod[:, 1].clone(), mod[:, 2].clone(), mod[:, 3].clone(), mod[:, 4].clone(), mod[:, 5].clone())
    h = _layernorm(x, axis=-1) * (1 + ca) + sa
    x = x + ga * _A_ref(h, A_dft_seq_re, A_dft_seq_im, A_dft_hid_re, A_dft_hid_im)
    h = _layernorm(x, axis=-1) * (1 + cb) + sb
    x = x + gb * _B_ref(h, B_w_r, B_w_gate, B_w_up, B_w_down)
    return x
```

```cpp
#include <hip/hip_runtime.h>
#include <hip/hip_bf16.h>
typedef unsigned short u16;
constexpr int S=2048, D=448, F=1792;
__device__ __forceinline__ float bf(u16 v){ return __uint_as_float(((unsigned)v)<<16); }
__device__ __forceinline__ u16 tobf(float f){ __hip_bfloat16 h = __float2bfloat16(f); return *(u16*)&h; }
__device__ __forceinline__ float rb(float f){ return bf(tobf(f)); }

__global__ void k_mod(const u16* c, const u16* wmod, float* mod){
  int j = blockIdx.x*blockDim.x+threadIdx.x; if (j>=6*D) return;
  float acc=0; for(int k=0;k<D;k++){ float cv=bf(c[k]); float s=rb(cv/(1.f+__expf(-cv))); acc+= s*bf(wmod[k*6*D+j]); }
  mod[j]=rb(acc);
}
__global__ void k_ln(const u16* x, u16* lnx){
  int s=blockIdx.x; int t=threadIdx.x; __shared__ float red[2][64];
  float a=0,b=0; for(int d=t;d<D;d+=64){float v=bf(x[s*D+d]); a+=v;}
  for(int o=32;o;o>>=1) a+=__shfl_xor(a,o);
  float m=a/D;
  for(int d=t;d<D;d+=64){float v=bf(x[s*D+d])-m; b+=v*v;}
  for(int o=32;o;o>>=1) b+=__shfl_xor(b,o);
  float rs=rsqrtf(b/D+1e-5f);
  for(int d=t;d<D;d+=64){ lnx[s*D+d]=tobf((bf(x[s*D+d])-m)*rs);}
}
__global__ void k_big(const u16* wre, const u16* wim, const u16* lnx, const float* mod, u16* pq){
  int t=blockIdx.x; int d=threadIdx.x;
  float p=0,q=0,rr=0,ri=0;
  for(int s=0;s<S;s++){ float v=bf(lnx[s*D+d]); float a=bf(wre[t*S+s]), b=bf(wim[t*S+s]); p+=a*v; q+=b*v; rr+=a; ri+=b;}
  float sa=mod[d], ca=1.f+mod[D+d];
  pq[t*2*D+d]=tobf(p*ca+rr*sa); pq[t*2*D+D+d]=tobf(q*ca+ri*sa);
}
__global__ void k_small(const u16* pq, const u16* dre, const u16* dim_, const u16* x, const float* mod, const u16* wr, u16* out, float* r, float* h2o, float* x1o){
  int t=blockIdx.x; int e=threadIdx.x; __shared__ float red[8];
  float y=0; for(int d=0;d<D;d++){ y+= bf(pq[t*2*D+d])*bf(dre[e*D+d]) - bf(pq[t*2*D+D+d])*bf(dim_[e*D+d]); }
  float x1=bf(x[t*D+e])+mod[2*D+e]*y;
  float a=x1; for(int o=32;o;o>>=1) a+=__shfl_xor(a,o);
  if((e&63)==0) red[e>>6]=a; __syncthreads();
  float m=0; for(int i=0;i<7;i++) m+=red[i]; m/=D; __syncthreads();
  float b=(x1-m)*(x1-m); for(int o=32;o;o>>=1) b+=__shfl_xor(b,o);
  if((e&63)==0) red[e>>6]=b; __syncthreads();
  float v=0; for(int i=0;i<7;i++) v+=red[i]; v/=D; __syncthreads();
  float h2=(x1-m)*rsqrtf(v+1e-5f)*(1.f+mod[4*D+e])+mod[3*D+e];
  float c=h2*bf(wr[e]); for(int o=32;o;o>>=1) c+=__shfl_xor(c,o);
  if((e&63)==0) red[e>>6]=c; __syncthreads();
  if(e==0){ float s=0; for(int i=0;i<7;i++) s+=red[i]; r[t]=s; }
  h2o[t*D+e]=h2; x1o[t*D+e]=x1;
  out[t*D+e]=tobf(x1+mod[5*D+e]*h2);
}
__global__ void k_mlp1(const float* r, const float* h2, const u16* wg, const u16* wu, float* gu, int* idxo){
  __shared__ float sv[256]; __shared__ int si[256];
  int t=threadIdx.x; float best=-1e30f; int bi=0;
  for(int i=t;i<S;i+=256){ if(r[i]>best){best=r[i];bi=i;} }
  sv[t]=best; si[t]=bi; __syncthreads();
  for(int o=128;o;o>>=1){ if(t<o){ if(sv[t+o]>sv[t] || (sv[t+o]==sv[t]&&si[t+o]<si[t])){sv[t]=sv[t+o];si[t]=si[t+o];} } __syncthreads(); }
  int idx=si[0];
  int f=blockIdx.x*256+t; float g=0,u=0;
  for(int d=0;d<D;d++){ float h=rb(h2[idx*D+d]); g+=h*bf(wg[d*F+f]); u+=h*bf(wu[d*F+f]); }
  g=rb(g); u=rb(u);
  gu[f]=rb(rb(g/(1.f+__expf(-g)))*u);
  if(f==0) *idxo=idx;
}
__global__ void k_mlp2(const float* gu, const u16* wd, const int* idxp, const float* r, const float* h2, const float* x1, const float* mod, u16* out){
  int e=blockIdx.x*64+threadIdx.x; int idx=*idxp; float y=0;
  for(int f=0;f<F;f++) y+=gu[f]*bf(wd[f*D+e]);
  float sg=1.f/(1.f+__expf(-r[idx]));
  out[idx*D+e]=tobf(x1[idx*D+e]+mod[5*D+e]*(h2[idx*D+e]+y*sg));
}
extern "C" void kernel_launch(void* const* d_in, const int* in_sizes, int n_in,
                              void* d_out, int out_size, void* d_ws, size_t ws_size,
                              hipStream_t stream) {
  const u16* x=(const u16*)d_in[0]; const u16* wre=(const u16*)d_in[1]; const u16* wim=(const u16*)d_in[2];
  const u16* dre=(const u16*)d_in[3]; const u16* dim_=(const u16*)d_in[4];
  const u16* wr=(const u16*)d_in[6]; const u16* wg=(const u16*)d_in[7]; const u16* wu=(const u16*)d_in[8]; const u16* wd=(const u16*)d_in[9];
  const u16* c=(const u16*)d_in[10]; const u16* wmod=(const u16*)d_in[11];
  char* ws=(char*)d_ws;
  float* mod=(float*)ws; u16* lnx=(u16*)(ws+(1<<20)); u16* pq=(u16*)(ws+(4<<20)); float* r=(float*)(ws+(8<<20)); float* h2=(float*)(ws+(9<<20)); float* x1=(float*)(ws+(16<<20));
  float* gu=(float*)(ws+(24<<20)); int* idx=(int*)(ws+(25<<20));
  k_mod<<<(6*D+63)/64,64,0,stream>>>(c,wmod,mod);
  k_ln<<<S,64,0,stream>>>(x,lnx);
  k_big<<<S,D,0,stream>>>(wre,wim,lnx,mod,pq);
  k_small<<<S,D,0,stream>>>(pq,dre,dim_,x,mod,wr,(u16*)d_out,r,h2,x1);
  k_mlp1<<<F/256,256,0,stream>>>(r,h2,wg,wu,gu,idx);
  k_mlp2<<<D/64,64,0,stream>>>(gu,wd,idx,r,h2,x1,mod,(u16*)d_out);
}
```

```cpp
#include <hip/hip_runtime.h>
#include <hip/hip_bf16.h>
typedef unsigned short u16;
constexpr int S=2048, D=448, F=1792;
__device__ __forceinline__ float bf(u16 v){ return __uint_as_float(((unsigned)v)<<16); }
__device__ __forceinline__ u16 tobf(float f){ __hip_bfloat16 h = __float2bfloat16(f); return *(u16*)&h; }
__device__ __forceinline__ float rb(float f){ return bf(tobf(f)); }

__global__ void k_mod(const u16* c, const u16* wmod, float* mod){
  int j = blockIdx.x*blockDim.x+threadIdx.x; if (j>=6*D) return;
  float acc=0; for(int k=0;k<D;k++){ float cv=bf(c[k]); float s=rb(cv/(1.f+__expf(-cv))); acc+= s*bf(wmod[k*6*D+j]); }
  mod[j]=rb(acc);
}
__global__ void k_ln(const u16* x, u16* lnx){
  int s=blockIdx.x; int t=threadIdx.x; __shared__ float red[2][64];
  float a=0,b=0; for(int d=t;d<D;d+=64){float v=bf(x[s*D+d]); a+=v;}
  for(int o=32;o;o>>=1) a+=__shfl_xor(a,o);
  float m=a/D;
  for(int d=t;d<D;d+=64){float v=bf(x[s*D+d])-m; b+=v*v;}
  for(int o=32;o;o>>=1) b+=__shfl_xor(b,o);
  float rs=rsqrtf(b/D+1e-5f);
  for(int d=t;d<D;d+=64){ lnx[s*D+d]=tobf((bf(x[s*D+d])-m)*rs);}
}
__global__ void k_big(const u16* wre, const u16* wim, const u16* lnx, const float* mod, u16* pq){
  int t=blockIdx.x; int d=threadIdx.x;
  float p=0,q=0,rr=0,ri=0;
  for(int s=0;s<S;s++){ float v=bf(lnx[s*D+d]); float a=bf(wre[t*S+s]), b=bf(wim[t*S+s]); p+=a*v; q+=b*v; rr+=a; ri+=b;}
  float sa=mod[d], ca=1.f+mod[D+d];
  pq[t*2*D+d]=tobf(p*ca+rr*sa); pq[t*2*D+D+d]=tobf(q*ca+ri*sa);
}
__global__ void k_small(const u16* pq, const u16* dre, const u16* dim_, const u16* x, const float* mod, const u16* wr, u16* out, float* r, float* h2o, float* x1o){
  int t=blockIdx.x; int e=threadIdx.x; __shared__ float red[8];
  float y=0; for(int d=0;d<D;d++){ y+= bf(pq[t*2*D+d])*bf(dre[e*D+d]) - bf(pq[t*2*D+D+d])*bf(dim_[e*D+d]); }
  float x1=bf(x[t*D+e])+mod[2*D+e]*y;
  float a=x1; for(int o=32;o;o>>=1) a+=__shfl_xor(a,o);
  if((e&63)==0) red[e>>6]=a; __syncthreads();
  float m=0; for(int i=0;i<7;i++) m+=red[i]; m/=D; __syncthreads();
  float b=(x1-m)*(x1-m); for(int o=32;o;o>>=1) b+=__shfl_xor(b,o);
  if((e&63)==0) red[e>>6]=b; __syncthreads();
  float v=0; for(int i=0;i<7;i++) v+=red[i]; v/=D; __syncthreads();
  float h2=(x1-m)*rsqrtf(v+1e-5f)*(1.f+mod[4*D+e])+mod[3*D+e];
  float c=h2*bf(wr[e]); for(int o=32;o;o>>=1) c+=__shfl_xor(c,o);
  if((e&63)==0) red[e>>6]=c; __syncthreads();
  if(e==0){ float s=0; for(int i=0;i<7;i++) s+=red[i]; r[t]=s; }
  h2o[t*D+e]=h2; x1o[t*D+e]=x1;
  out[t*D+e]=tobf(x1+mod[5*D+e]*h2);
}
__global__ void k_mlp1(const float* r, const float* h2, const u16* wg, const u16* wu, float* gu, int* idxo){
  __shared__ float sv[256]; __shared__ int si[256];
  int t=threadIdx.x; float best=-1e30f; int bi=0;
  for(int i=t;i<S;i+=256){ if(r[i]>best){best=r[i];bi=i;} }
  sv[t]=best; si[t]=bi; __syncthreads();
  for(int o=128;o;o>>=1){ if(t<o){ if(sv[t+o]>sv[t] || (sv[t+o]==sv[t]&&si[t+o]<si[t])){sv[t]=sv[t+o];si[t]=si[t+o];} } __syncthreads(); }
  int idx=si[0];
  int f=blockIdx.x*256+t; float g=0,u=0;
  for(int d=0;d<D;d++){ float h=rb(h2[idx*D+d]); g+=h*bf(wg[d*F+f]); u+=h*bf(wu[d*F+f]); }
  g=rb(g); u=rb(u);
  gu[f]=rb(rb(g/(1.f+__expf(-g)))*u);
  if(f==0) *idxo=idx;
}
__global__ void k_mlp2(const float* gu, const u16* wd, const int* idxp, const float* r, const float* h2, const float* x1, const float* mod, u16* out){
  int e=blockIdx.x*64+threadIdx.x; int idx=*idxp; float y=0;
  for(int f=0;f<F;f++) y+=gu[f]*bf(wd[f*D+e]);
  float sg=1.f/(1.f+__expf(-r[idx]));
  out[idx*D+e]=tobf(x1[idx*D+e]+mod[5*D+e]*(h2[idx*D+e]+y*sg));
}

typedef unsigned long long u64;
typedef int v4i __attribute__((ext_vector_type(4)));
__device__ __forceinline__ u64 rt() { return __builtin_amdgcn_s_memrealtime(); }
__device__ __forceinline__ v4i mk_srd(const void* p, unsigned bytes) { u64 a = (u64)p; v4i r = {(int)(unsigned)a, (int)(unsigned)(a >> 32) & 0xffff, (int)bytes, 0x00020000}; return r; }
template <int R, int IMM, int AUX> __device__ __forceinline__ void lda(v4i srd, int voff, int soff) {
  if (AUX == 0) asm volatile("buffer_load_dwordx4 a[%c0:%c1], %2, %3, %4 offen offset:%c5" :: "n"(R), "n"(R + 3), "v"(voff), "s"(srd), "s"(soff), "n"(IMM) : "memory");
  else asm volatile("buffer_load_dwordx4 a[%c0:%c1], %2, %3, %4 offen offset:%c5 nt" :: "n"(R), "n"(R + 3), "v"(voff), "s"(srd), "s"(soff), "n"(IMM) : "memory");
}
template <int N, int AUX, int I> __device__ __forceinline__ void ld_all(v4i rr, v4i ri, int voff, int s0) {
  if constexpr (I < N) {
    constexpr int i = I >> 1; lda<I * 4, 0, AUX>((I & 1) ? ri : rr, voff, s0 + i * 4096);
    ld_all<N, AUX, I + 1>(rr, ri, voff, s0);
  }
}
template <int N, int AUX>
__global__ void __launch_bounds__(256, 1) k_probe(const char* wre, const char* wim, u64* stamps) {
  u64 t0 = rt();
  asm volatile("" ::: "a0","a1","a2","a3","a4","a5","a6","a7","a8","a9","a10","a11","a12","a13","a14","a15","a16","a17","a18","a19","a20","a21","a22","a23","a24","a25","a26","a27","a28","a29","a30","a31","a32","a33","a34","a35","a36","a37","a38","a39","a40","a41","a42","a43","a44","a45","a46","a47","a48","a49","a50","a51","a52","a53","a54","a55","a56","a57","a58","a59","a60","a61","a62","a63","a64","a65","a66","a67","a68","a69","a70","a71","a72","a73","a74","a75","a76","a77","a78","a79","a80","a81","a82","a83","a84","a85","a86","a87","a88","a89","a90","a91","a92","a93","a94","a95","a96","a97","a98","a99","a100","a101","a102","a103","a104","a105","a106","a107","a108","a109","a110","a111","a112","a113","a114","a115","a116","a117","a118","a119","a120","a121","a122","a123","a124","a125","a126","a127","a128","a129","a130","a131","a132","a133","a134","a135","a136","a137","a138","a139","a140","a141","a142","a143","a144","a145","a146","a147","a148","a149","a150","a151","a152","a153","a154","a155","a156","a157","a158","a159","a160","a161","a162","a163","a164","a165","a166","a167","a168","a169","a170","a171","a172","a173","a174","a175","a176","a177","a178","a179","a180","a181","a182","a183","a184","a185","a186","a187","a188","a189","a190","a191","a192","a193","a194","a195","a196","a197","a198","a199","a200","a201","a202","a203","a204","a205","a206","a207","a208","a209","a210","a211","a212","a213","a214","a215","a216","a217","a218","a219","a220","a221","a222","a223","a224","a225","a226","a227","a228","a229","a230","a231","a232","a233","a234","a235","a236","a237","a238","a239","a240","a241","a242","a243","a244","a245","a246","a247","a248","a249","a250","a251","a252","a253","a254","a255");
  int b = blockIdx.x; int g = (b & 7) + 8 * ((b >> 3) >> 2);
  int wave = __builtin_amdgcn_readfirstlane(threadIdx.x >> 6), lane = threadIdx.x & 63;
  v4i rr = mk_srd(wre, 2048 * 2048 * 2), ri = mk_srd(wim, 2048 * 2048 * 2);
  int voff = (g * 32) * 4096 + wave * 1024 + lane * 16;
  asm volatile("s_nop 4");
  ld_all<N, AUX, 0>(rr, ri, voff, 0);
  asm volatile("s_waitcnt vmcnt(0)" ::: "memory");
  u64 t2 = rt();
  if (lane == 0) { u64* s = stamps + (b * 4 + wave) * 2; s[0] = t0; s[1] = t2; }
}
__global__ void k_spin(const u64* stamps, int mode) {
  u64 mn = ~0ull, mx0 = 0, mx = 0, sum = 0;
  for (int i = 0; i < 1024; i++) { u64 a = stamps[i * 2], b = stamps[i * 2 + 1]; mn = a < mn ? a : mn; mx0 = a > mx0 ? a : mx0; mx = b > mx ? b : mx; sum += b - a; }
  u64 d = mode == 0 ? mx - mn : mode == 1 ? mx0 - mn : sum / 1024;
  if (d > 3000) d = 3000;
  u64 t = rt();
  while (rt() - t < d * 500) __builtin_amdgcn_s_sleep(4);
}
extern "C" void kernel_launch(void* const* d_in, const int* in_sizes, int n_in,
                              void* d_out, int out_size, void* d_ws, size_t ws_size,
                              hipStream_t stream) {
  const u16* x=(const u16*)d_in[0]; const u16* wre=(const u16*)d_in[1]; const u16* wim=(const u16*)d_in[2];
  const u16* dre=(const u16*)d_in[3]; const u16* dim_=(const u16*)d_in[4];
  const u16* wr=(const u16*)d_in[6]; const u16* wg=(const u16*)d_in[7]; const u16* wu=(const u16*)d_in[8]; const u16* wd=(const u16*)d_in[9];
  const u16* c=(const u16*)d_in[10]; const u16* wmod=(const u16*)d_in[11];
  char* ws=(char*)d_ws;
  float* mod=(float*)ws; u16* lnx=(u16*)(ws+(1<<20)); u16* pq=(u16*)(ws+(4<<20)); float* r=(float*)(ws+(8<<20)); float* h2=(float*)(ws+(9<<20)); float* x1=(float*)(ws+(16<<20));
  float* gu=(float*)(ws+(24<<20)); int* idx=(int*)(ws+(25<<20));
  u64* stamps=(u64*)(ws+(30<<20));
  k_probe<2,0><<<256,256,0,stream>>>((const char*)wre,(const char*)wim,stamps);
  k_mod<<<(6*D+63)/64,64,0,stream>>>(c,wmod,mod);
  k_ln<<<S,64,0,stream>>>(x,lnx);
  k_big<<<S,D,0,stream>>>(wre,wim,lnx,mod,pq);
  k_small<<<S,D,0,stream>>>(pq,dre,dim_,x,mod,wr,(u16*)d_out,r,h2,x1);
  k_mlp1<<<F/256,256,0,stream>>>(r,h2,wg,wu,gu,idx);
  k_mlp2<<<D/64,64,0,stream>>>(gu,wd,idx,r,h2,x1,mod,(u16*)d_out);
  k_spin<<<1,1,0,stream>>>(stamps,1);
}
```

```cpp
#include <hip/hip_runtime.h>
#include <hip/hip_bf16.h>
typedef unsigned short u16;
constexpr int S=2048, D=448, F=1792;
__device__ __forceinline__ float bf(u16 v){ return __uint_as_float(((unsigned)v)<<16); }
__device__ __forceinline__ u16 tobf(float f){ __hip_bfloat16 h = __float2bfloat16(f); return *(u16*)&h; }
__device__ __forceinline__ float rb(float f){ return bf(tobf(f)); }

__global__ void k_mod(const u16* c, const u16* wmod, float* mod){
  int j = blockIdx.x*blockDim.x+threadIdx.x; if (j>=6*D) return;
  float acc=0; for(int k=0;k<D;k++){ float cv=bf(c[k]); float s=rb(cv/(1.f+__expf(-cv))); acc+= s*bf(wmod[k*6*D+j]); }
  mod[j]=rb(acc);
}
__global__ void k_ln(const u16* x, u16* lnx){
  int s=blockIdx.x; int t=threadIdx.x; __shared__ float red[2][64];
  float a=0,b=0; for(int d=t;d<D;d+=64){float v=bf(x[s*D+d]); a+=v;}
  for(int o=32;o;o>>=1) a+=__shfl_xor(a,o);
  float m=a/D;
  for(int d=t;d<D;d+=64){float v=bf(x[s*D+d])-m; b+=v*v;}
  for(int o=32;o;o>>=1) b+=__shfl_xor(b,o);
  float rs=rsqrtf(b/D+1e-5f);
  for(int d=t;d<D;d+=64){ lnx[s*D+d]=tobf((bf(x[s*D+d])-m)*rs);}
}
__global__ void k_big(const u16* wre, const u16* wim, const u16* lnx, const float* mod, u16* pq){
  int t=blockIdx.x; int d=threadIdx.x;
  float p=0,q=0,rr=0,ri=0;
  for(int s=0;s<S;s++){ float v=bf(lnx[s*D+d]); float a=bf(wre[t*S+s]), b=bf(wim[t*S+s]); p+=a*v; q+=b*v; rr+=a; ri+=b;}
  float sa=mod[d], ca=1.f+mod[D+d];
  pq[t*2*D+d]=tobf(p*ca+rr*sa); pq[t*2*D+D+d]=tobf(q*ca+ri*sa);
}
__global__ void k_small(const u16* pq, const u16* dre, const u16* dim_, const u16* x, const float* mod, const u16* wr, u16* out, float* r, float* h2o, float* x1o){
  int t=blockIdx.x; int e=threadIdx.x; __shared__ float red[8];
  float y=0; for(int d=0;d<D;d++){ y+= bf(pq[t*2*D+d])*bf(dre[e*D+d]) - bf(pq[t*2*D+D+d])*bf(dim_[e*D+d]); }
  float x1=bf(x[t*D+e])+mod[2*D+e]*y;
  float a=x1; for(int o=32;o;o>>=1) a+=__shfl_xor(a,o);
  if((e&63)==0) red[e>>6]=a; __syncthreads();
  float m=0; for(int i=0;i<7;i++) m+=red[i]; m/=D; __syncthreads();
  float b=(x1-m)*(x1-m); for(int o=32;o;o>>=1) b+=__shfl_xor(b,o);
  if((e&63)==0) red[e>>6]=b; __syncthreads();
  float v=0; for(int i=0;i<7;i++) v+=red[i]; v/=D; __syncthreads();
  float h2=(x1-m)*rsqrtf(v+1e-5f)*(1.f+mod[4*D+e])+mod[3*D+e];
  float c=h2*bf(wr[e]); for(int o=32;o;o>>=1) c+=__shfl_xor(c,o);
  if((e&63)==0) red[e>>6]=c; __syncthreads();
  if(e==0){ float s=0; for(int i=0;i<7;i++) s+=red[i]; r[t]=s; }
  h2o[t*D+e]=h2; x1o[t*D+e]=x1;
  out[t*D+e]=tobf(x1+mod[5*D+e]*h2);
}
__global__ void k_mlp1(const float* r, const float* h2, const u16* wg, const u16* wu, float* gu, int* idxo){
  __shared__ float sv[256]; __shared__ int si[256];
  int t=threadIdx.x; float best=-1e30f; int bi=0;
  for(int i=t;i<S;i+=256){ if(r[i]>best){best=r[i];bi=i;} }
  sv[t]=best; si[t]=bi; __syncthreads();
  for(int o=128;o;o>>=1){ if(t<o){ if(sv[t+o]>sv[t] || (sv[t+o]==sv[t]&&si[t+o]<si[t])){sv[t]=sv[t+o];si[t]=si[t+o];} } __syncthreads(); }
  int idx=si[0];
  int f=blockIdx.x*256+t; float g=0,u=0;
  for(int d=0;d<D;d++){ float h=rb(h2[idx*D+d]); g+=h*bf(wg[d*F+f]); u+=h*bf(wu[d*F+f]); }
  g=rb(g); u=rb(u);
  gu[f]=rb(rb(g/(1.f+__expf(-g)))*u);
  if(f==0) *idxo=idx;
}
__global__ void k_mlp2(const float* gu, const u16* wd, const int* idxp, const float* r, const float* h2, const float* x1, const float* mod, u16* out){
  int e=blockIdx.x*64+threadIdx.x; int idx=*idxp; float y=0;
  for(int f=0;f<F;f++) y+=gu[f]*bf(wd[f*D+e]);
  float sg=1.f/(1.f+__expf(-r[idx]));
  out[idx*D+e]=tobf(x1[idx*D+e]+mod[5*D+e]*(h2[idx*D+e]+y*sg));
}

typedef unsigned long long u64;
typedef unsigned v4u __attribute__((ext_vector_type(4)));
__device__ __forceinline__ u64 rt() { u64 t; asm volatile("s_memrealtime %0\n\ts_waitcnt lgkmcnt(0)" : "=s"(t) :: "memory"); return t; }
template <int N>
__global__ void __launch_bounds__(256, 1) k_a(v4u* mb, u64* stamps) {
  int b = blockIdx.x; int wave = threadIdx.x >> 6, lane = threadIdx.x & 63;
  u64 t0 = rt();
  while (rt() - t0 < 300) __builtin_amdgcn_s_sleep(1);
  u64 t1 = rt();
  v4u v = {1u, (unsigned)lane, 3u, (unsigned)b};
#pragma unroll
  for (int i = 0; i < N; i++) mb[(b * N + i) * 256 + threadIdx.x] = v;
  if (lane == 0) { u64* s = stamps + (b * 4 + wave) * 4; s[0] = t1; }
}
template <int N>
__global__ void __launch_bounds__(256, 1) k_b(const v4u* mb, u64* stamps, unsigned* sink) {
  u64 t0 = rt();
  int b = blockIdx.x; int wave = threadIdx.x >> 6, lane = threadIdx.x & 63;
  unsigned x = 0; int src = (b + 8) & 255;
#pragma unroll
  for (int i = 0; i < N; i++) { v4u v = mb[(src * N + i) * 256 + threadIdx.x]; x += v.x ^ v.y ^ v.z ^ v.w; }
  asm volatile("" : "+v"(x));
  u64 t1 = rt();
  if (x == 0x12345678) sink[0] = x;
  if (lane == 0) { u64* s = stamps + (b * 4 + wave) * 4; s[1] = t0; s[2] = t1; }
}
__global__ void k_spin(const u64* stamps, int mode) {
  u64 mxa = 0, mnb = ~0ull, mxb = 0, mxc = 0;
  for (int i = 0; i < 1024; i++) { u64 a = stamps[i * 4], b = stamps[i * 4 + 1], c = stamps[i * 4 + 2]; mxa = a > mxa ? a : mxa; mnb = b < mnb ? b : mnb; mxb = b > mxb ? b : mxb; mxc = c > mxc ? c : mxc; }
  u64 d = mode == 0 ? mnb - mxa : mode == 1 ? mxb - mxa : mxc - mxa;
  if (d > 3000) d = 3000;
  u64 t = rt();
  while (rt() - t < d * 500) __builtin_amdgcn_s_sleep(4);
}
extern "C" void kernel_launch(void* const* d_in, const int* in_sizes, int n_in,
                              void* d_out, int out_size, void* d_ws, size_t ws_size,
                              hipStream_t stream) {
  const u16* x=(const u16*)d_in[0]; const u16* wre=(const u16*)d_in[1]; const u16* wim=(const u16*)d_in[2];
  const u16* dre=(const u16*)d_in[3]; const u16* dim_=(const u16*)d_in[4];
  const u16* wr=(const u16*)d_in[6]; const u16* wg=(const u16*)d_in[7]; const u16* wu=(const u16*)d_in[8]; const u16* wd=(const u16*)d_in[9];
  const u16* c=(const u16*)d_in[10]; const u16* wmod=(const u16*)d_in[11];
  char* ws=(char*)d_ws;
  float* mod=(float*)ws; u16* lnx=(u16*)(ws+(1<<20)); u16* pq=(u16*)(ws+(4<<20)); float* r=(float*)(ws+(8<<20)); float* h2=(float*)(ws+(9<<20)); float* x1=(float*)(ws+(16<<20));
  float* gu=(float*)(ws+(24<<20)); int* idx=(int*)(ws+(25<<20));
  u64* stamps=(u64*)(ws+(30<<20));
  k_a<0><<<256,256,0,stream>>>((v4u*)(ws+(64<<20)),stamps);
  k_b<0><<<256,256,0,stream>>>((v4u*)(ws+(64<<20)),stamps,(unsigned*)(ws+(31<<20)));
  k_mod<<<(6*D+63)/64,64,0,stream>>>(c,wmod,mod);
  k_ln<<<S,64,0,stream>>>(x,lnx);
  k_big<<<S,D,0,stream>>>(wre,wim,lnx,mod,pq);
  k_small<<<S,D,0,stream>>>(pq,dre,dim_,x,mod,wr,(u16*)d_out,r,h2,x1);
  k_mlp1<<<F/256,256,0,stream>>>(r,h2,wg,wu,gu,idx);
  k_mlp2<<<D/64,64,0,stream>>>(gu,wd,idx,r,h2,x1,mod,(u16*)d_out);
  k_spin<<<1,1,0,stream>>>(stamps,0);
}
```

```cpp
#include <hip/hip_runtime.h>
#include <hip/hip_bf16.h>

typedef unsigned u32;
typedef unsigned long long u64;
typedef u32 v4u __attribute__((ext_vector_type(4)));
typedef u32 v2u __attribute__((ext_vector_type(2)));
typedef __bf16 bfx2 __attribute__((ext_vector_type(2)));
typedef __bf16 bfx8 __attribute__((ext_vector_type(8)));
typedef float f32x4 __attribute__((ext_vector_type(4)));
typedef float f32x2 __attribute__((ext_vector_type(2)));
typedef __amdgpu_buffer_rsrc_t rsrc_t;
#define LAS __attribute__((address_space(3)))
#define DEV __device__ __forceinline__
#define FENCE() asm volatile("" ::: "memory")
#define WAITVM(n) asm volatile("s_waitcnt vmcnt(" #n ")" ::: "memory")
#define WAITLGKM0() asm volatile("s_waitcnt lgkmcnt(0)" ::: "memory")
#define SCHED() __builtin_amdgcn_sched_barrier(0)

namespace {
constexpr int S = 2048, D = 448, F = 1792;
constexpr int SC1 = 16;
constexpr u32 OOB = 0x7ff00000u;
constexpr int SPIN = 1 << 17;
constexpr u32 WS_MODG = 4096;
constexpr u32 WS_BF = 16384;
constexpr u32 WS_CF = 49152;
constexpr u32 WS_DF = 81920;
constexpr u32 WS_DG = 114688;
constexpr u32 WS_DH = 131072;
constexpr u32 WS_DB = WS_DH + 256 * 2048;
constexpr u32 WS_EG = WS_DB + 256 * 2048;
constexpr u32 WS_AB = 2u << 20;
constexpr u32 WS_CM = 6u << 20;
constexpr u32 WS_DBG = 14u << 20;
constexpr u32 WS_SIZE = 16u << 20;
constexpr u32 L_WD = 0, L_X = 100352, L_MP = 157696, L_CNT = 159488, L_MPART = 159552, L_MR = 159808, L_RB = 163392, L_RB2 = 163520;
constexpr u32 L_RED = 131072, L_Y = 131072, L_GU = 0, L_DN = 4096;
constexpr u32 L_SIZE = 163840;
constexpr int NMLP = 56;

DEV rsrc_t mk(const void* p, u32 bytes) { return __builtin_amdgcn_make_buffer_rsrc((void*)p, 0, bytes, 0x00020000); }
DEV v4u ld16(rsrc_t r, u32 v, u32 s, int aux) { return aux ? __builtin_amdgcn_raw_buffer_load_b128(r, v, s, SC1) : __builtin_amdgcn_raw_buffer_load_b128(r, v, s, 0); }
DEV void st16(v4u d, rsrc_t r, u32 v, u32 s) {
  __builtin_amdgcn_raw_buffer_store_b128(d, r, v, s, SC1);
  asm volatile("s_nop 1" :: "v"(d) : "memory");
}
DEV float lo(u32 w) { return __builtin_bit_cast(float, w << 16); }
DEV float hi(u32 w) { return __builtin_bit_cast(float, w & 0xffff0000u); }
DEV u32 pk(float a, float b) { f32x2 v = {a, b}; return __builtin_bit_cast(u32, __builtin_convertvector(v, bfx2)); }
DEV bfx2 bf2(u32 w) { return __builtin_bit_cast(bfx2, w); }
DEV float rbf(float a) { return lo(pk(a, a)); }
DEV u32 fu(float a) { return __builtin_bit_cast(u32, a); }
DEV float uf(u32 a) { return __builtin_bit_cast(float, a); }
template <int C> DEV float dpp(float v) { return uf(__builtin_amdgcn_update_dpp(0u, fu(v), C, 0xf, 0xf, true)); }
constexpr int QP1 = 0xB1, QP2 = 0x4E, ROR4 = 0x124, ROR8 = 0x128;
DEV float sum16(float a, float b) { auto s = __builtin_amdgcn_permlane16_swap(fu(a), fu(b), false, false); return uf(s[0]) + uf(s[1]); }
DEV float sum32(float a, float b) { auto s = __builtin_amdgcn_permlane32_swap(fu(a), fu(b), false, false); return uf(s[0]) + uf(s[1]); }
DEV float max16(float a) { auto s = __builtin_amdgcn_permlane16_swap(fu(a), fu(a), false, false); return fmaxf(uf(s[0]), uf(s[1])); }
DEV float max32(float a) { auto s = __builtin_amdgcn_permlane32_swap(fu(a), fu(a), false, false); return fmaxf(uf(s[0]), uf(s[1])); }
DEV float wsum(float v) { v += dpp<QP1>(v); v += dpp<QP2>(v); v += dpp<ROR4>(v); v += dpp<ROR8>(v); v = sum16(v, v); return sum32(v, v); }
DEV float wmax(float v) { v = fmaxf(v, dpp<QP1>(v)); v = fmaxf(v, dpp<QP2>(v)); v = fmaxf(v, dpp<ROR4>(v)); v = fmaxf(v, dpp<ROR8>(v)); return max32(max16(v)); }
DEV float sigm(float v) { return __builtin_amdgcn_rcpf(1.f + __expf(-v)); }
DEV bool tagok(v4u g, u64 ep) { return g.z == (u32)ep && g.w == (u32)(ep >> 32); }
DEV bool allok(bool p) { return __builtin_amdgcn_ballot_w64(!p) == 0; }
DEV f32x4 mfma(v4u a, v4u b, f32x4 c) { return __builtin_amdgcn_mfma_f32_16x16x32_bf16(__builtin_bit_cast(bfx8, a), __builtin_bit_cast(bfx8, b), c, 0, 0, 0); }
DEV void barrier() { FENCE(); __builtin_amdgcn_s_barrier(); FENCE(); }

DEV void dma_rows2(u32 m, u32 v, rsrc_t r, u32 s) {
  asm volatile("s_mov_b32 m0, %0\n\ts_nop 0\n\tbuffer_load_dwordx4 %1, %2, %3 offen lds\n\tbuffer_load_dwordx4 %1, %2, %3 offen offset:896 lds"
               :: "s"(m), "v"(v), "s"(r), "s"(s) : "memory");
}
DEV void dma4k(u32 m, u32 v, rsrc_t r, u32 s) {
  asm volatile("s_mov_b32 m0, %0\n\ts_nop 0\n\tbuffer_load_dwordx4 %1, %2, %3 offen sc1 lds\n\tbuffer_load_dwordx4 %1, %2, %3 offen offset:1024 sc1 lds\n\t"
               "buffer_load_dwordx4 %1, %2, %3 offen offset:2048 sc1 lds\n\tbuffer_load_dwordx4 %1, %2, %3 offen offset:3072 sc1 lds"
               :: "s"(m), "v"(v), "s"(r), "s"(s) : "memory");
}
DEV void tr2(u32& a0, u32& a1, u32& b0, u32& b1, u32& c0, u32& c1, u32& d0, u32& d1, u32& t0, u32& t1, u32& u0, u32& u1, u64 m5, u64 m3) {
#define CND(d, x, y, q) "v_cndmask_b32_dpp " d ", " x ", " y ", vcc quad_perm:" q " row_mask:0xf bank_mask:0xf\n\t"
#define Q1 "[1,0,3,2]"
#define Q2 "[2,3,0,1]"
  asm volatile("s_mov_b64 vcc, %12\n\ts_nop 0\n\t"
    CND("%8", "%2", "%0", Q1) CND("%9", "%3", "%1", Q1) CND("%10", "%6", "%4", Q1) CND("%11", "%7", "%5", Q1)
    "s_not_b64 vcc, vcc\n\t"
    CND("%2", "%0", "%2", Q1) CND("%3", "%1", "%3", Q1) CND("%6", "%4", "%6", Q1) CND("%7", "%5", "%7", Q1)
    "s_mov_b64 vcc, %13\n\t"
    CND("%0", "%10", "%8", Q2) CND("%1", "%11", "%9", Q2) CND("%4", "%6", "%2", Q2) CND("%5", "%7", "%3", Q2)
    "s_not_b64 vcc, vcc\n\t"
    CND("%10", "%8", "%10", Q2) CND("%11", "%9", "%11", Q2) CND("%6", "%2", "%6", Q2) CND("%7", "%3", "%7", Q2)
    : "+v"(a0), "+v"(a1), "+v"(b0), "+v"(b1), "+v"(c0), "+v"(c1), "+v"(d0), "+v"(d1), "=&v"(t0), "=&v"(t1), "=&v"(u0), "=&v"(u1)
    : "s"(m5), "s"(m3) : "vcc", "scc");
}
DEV void tr4(const v4u* g, v4u* f) {
  const u64 m5 = 0x5555555555555555ull, m3 = 0x3333333333333333ull;
  u32 a[4] = {g[0].x, g[0].y, g[0].z, g[0].w}, b[4] = {g[1].x, g[1].y, g[1].z, g[1].w};
  u32 c[4] = {g[2].x, g[2].y, g[2].z, g[2].w}, d[4] = {g[3].x, g[3].y, g[3].z, g[3].w}, t[4], u[4];
  tr2(a[0], a[1], b[0], b[1], c[0], c[1], d[0], d[1], t[0], t[1], u[0], u[1], m5, m3);
  tr2(a[2], a[3], b[2], b[3], c[2], c[3], d[2], d[3], t[2], t[3], u[2], u[3], m5, m3);
  f[0] = (v4u){a[0], a[1], a[2], a[3]}; f[1] = (v4u){c[0], c[1], c[2], c[3]};
  f[2] = (v4u){u[0], u[1], u[2], u[3]}; f[3] = (v4u){d[0], d[1], d[2], d[3]};
}
DEV u32 lds_inc(LAS char* L, u32 off) {
  FENCE(); u32 o = 0;
  if ((threadIdx.x & 63) == 0) o = __hip_atomic_fetch_add((LAS u32*)(L + off), 1u, __ATOMIC_RELAXED, __HIP_MEMORY_SCOPE_WORKGROUP);
  FENCE(); return __builtin_amdgcn_readfirstlane(o);
}
DEV void lds_wait(LAS char* L, u32 off, u32 n) {
  for (int it = 0; it < (1 << 22); it++) { FENCE(); if (*(volatile LAS u32*)(L + off) >= n) break; }
  FENCE();
}
#define TC(i)
#define TS(i)

__global__ void __launch_bounds__(512, 2) fused_kernel(const char* __restrict__ x, const char* __restrict__ wre, const char* __restrict__ wim,
    const char* __restrict__ dre, const char* __restrict__ dim, const char* __restrict__ wr, const char* __restrict__ wg,
    const char* __restrict__ wu, const char* __restrict__ wdn, const char* __restrict__ cv, const char* __restrict__ wmod,
    char* out, char* ws) {
  extern __shared__ __attribute__((aligned(16))) char smem[];
  LAS char* L = (LAS char*)smem;
  TS(0); TC(30);
  const int b = blockIdx.x, xcd = b & 7, r = b >> 3, j = r & 3;
  const int wave = __builtin_amdgcn_readfirstlane(threadIdx.x >> 6), lane = threadIdx.x & 63;
  const int tw = wave & 3, kw = wave >> 2;
  const int kq = xcd & 3, tq = (xcd >> 2) * 8 + (r >> 2);
  const int hp = xcd & 1, sp = (xcd >> 1) * 8 + (r >> 2);
  const bool comp = kw == (j >> 1);
  asm volatile("" :: "s"(x), "s"(wre), "s"(wim), "s"(dre), "s"(dim), "s"(wr), "s"(wg), "s"(wu), "s"(wdn), "s"(cv), "s"(wmod), "s"(out), "s"(ws));
  const rsrc_t rws = mk(ws, WS_SIZE);
#define LDEP(v) FENCE(); v2u v = __builtin_amdgcn_raw_buffer_load_b64(rws, 0, 0, SC1); FENCE()
#define GETEP(v) asm volatile("" : "+v"(v)); const u64 ep = (((u64)(u32)__builtin_amdgcn_readfirstlane(v.y) << 32) | (u32)__builtin_amdgcn_readfirstlane(v.x)) + 1
  const int q = lane >> 4, l15 = lane & 15;

  if (threadIdx.x < 8) *(LAS u32*)(L + L_CNT + threadIdx.x * 4) = 0;

  if (comp) {
    __builtin_amdgcn_s_setprio(2);
    {
      const rsrc_t rx = mk(x, S * D * 2), rd = mk(hp ? dim : dre, D * D * 2);
      if (lane < 56) {
        for (int i = 0; i < 8; i++) {
          int p = (i + j * 2) & 7;
          dma_rows2(L_X + tw * 14336 + p * 1792, (lane ^ p) * 16, rx, (sp * 64 + tw * 16 + p * 2) * 896);
        }
      }
      WAITLGKM0(); barrier();
      if (lane < 56) {
        const int rot = ((r >> 2) * 7) >> 2;
        for (int i = 0; i < 14; i++) {
          int p = i + rot; if (p >= 14) p -= 14;
          p += tw * 14;
          dma_rows2(L_WD + p * 1792, (lane ^ (p & 7)) * 16, rd, (j * 112 + p * 2) * 896);
        }
      }
    }
    LDEP(epc);
    TS(1);
    WAITVM(29);
    const u32 sw0 = l15 * 896 + ((q ^ (l15 >> 1)) << 4), sw1 = l15 * 896 + (((4 + q) ^ (l15 >> 1)) << 4);
    u32 xn[112];
    {
      float v[112];
#pragma unroll
      for (int kb = 0; kb < 14; kb++) {
        v4u t = *(LAS v4u*)(L + L_X + tw * 14336 + ((kb & 1) ? sw1 : sw0) + (kb >> 1) * 128);
#pragma unroll
        for (int i = 0; i < 4; i++) { v[kb * 8 + 2 * i] = lo(t[i]); v[kb * 8 + 2 * i + 1] = hi(t[i]); }
      }
      f32x2 a0 = {0, 0}, a1 = {0, 0};
#pragma unroll
      for (int i = 0; i < 112; i += 4) { a0 += (f32x2){v[i], v[i + 1]}; a1 += (f32x2){v[i + 2], v[i + 3]}; }
      a0 += a1;
      float m = a0.x + a0.y; m = sum16(m, m); m = sum32(m, m); m *= (1.f / D);
      a0 = (f32x2){0, 0}; a1 = a0;
#pragma unroll
      for (int i = 0; i < 112; i += 4) {
        f32x2 d0 = (f32x2){v[i], v[i + 1]} - m, d1 = (f32x2){v[i + 2], v[i + 3]} - m;
        v[i] = d0.x; v[i + 1] = d0.y; v[i + 2] = d1.x; v[i + 3] = d1.y;
        a0 += d0 * d0; a1 += d1 * d1;
      }
      a0 += a1;
      float var = a0.x + a0.y; var = sum16(var, var); var = sum32(var, var);
      const float rs = __builtin_amdgcn_rsqf(var * (1.f / D) + 1e-5f);
#pragma unroll
      for (int i = 0; i < 112; i += 2) { f32x2 d = (f32x2){v[i], v[i + 1]} * rs; xn[i] = pk(d.x, 1.f); xn[i + 1] = pk(d.y, 1.f); }
    }
#pragma unroll
    for (int i = 0; i < 112; i++) asm volatile("" : "+v"(xn[i]));
    TS(2);
    WAITVM(0);
    GETEP(epc);
    lds_inc(L, L_CNT + 0);
    lds_wait(L, L_CNT + 0, 4);
    TS(3);
    v4u wf[2][7];
#pragma unroll
    for (int kb = 0; kb < 2; kb++)
#pragma unroll
      for (int et = 0; et < 7; et++) wf[kb][et] = *(LAS v4u*)(L + L_WD + et * 14336 + (kb ? sw1 : sw0));
    {
      const u32 go = (lane & 31) < 28 ? WS_MODG + ((lane >> 5) * 112 + tw * 28 + (lane & 31)) * 16 : OOB;
      v4u g;
      for (int it = 0; it < SPIN; it++) { FENCE(); g = ld16(rws, go, 0, 1); if (allok((lane & 31) >= 28 || tagok(g, ep))) break; }
      auto s0 = __builtin_amdgcn_permlane32_swap(g.x, g.x, false, false);
      auto s1 = __builtin_amdgcn_permlane32_swap(g.y, g.y, false, false);
      const u32 c0 = pk(1.f + lo(s0[1]), 1.f + hi(s0[1])), c1 = pk(1.f + lo(s1[1]), 1.f + hi(s1[1]));
      v4u o = {(c0 & 0xffffu) | (s0[0] << 16), (c0 >> 16) | (s0[0] & 0xffff0000u), (c1 & 0xffffu) | (s1[0] << 16), (c1 >> 16) | (s1[0] & 0xffff0000u)};
      if (lane < 28) *(LAS v4u*)(L + L_MP + (tw * 28 + lane) * 16) = o;
    }
    lds_inc(L, L_CNT + 4);
    lds_wait(L, L_CNT + 4, 4);
    TS(4);
    f32x4 acc[7];
    v4u mp[2][2];
    mp[0][0] = *(LAS v4u*)(L + L_MP + q * 32); mp[0][1] = *(LAS v4u*)(L + L_MP + q * 32 + 16);
#pragma unroll
    for (int kb = 0; kb < 14; kb++) {
      if (kb < 13) { mp[(kb + 1) & 1][0] = *(LAS v4u*)(L + L_MP + (kb + 1) * 128 + q * 32); mp[(kb + 1) & 1][1] = *(LAS v4u*)(L + L_MP + (kb + 1) * 128 + q * 32 + 16); }
      float h[8];
#pragma unroll
      for (int i = 0; i < 8; i++)
        h[i] = __builtin_amdgcn_fdot2_f32_bf16(bf2(xn[kb * 8 + i]), bf2(mp[kb & 1][i >> 2][i & 3]), 0.f, false);
      const v4u hf = {pk(h[0], h[1]), pk(h[2], h[3]), pk(h[4], h[5]), pk(h[6], h[7])};
#pragma unroll
      for (int et = 0; et < 7; et++) {
        acc[et] = mfma(hf, wf[kb & 1][et], kb == 0 ? (f32x4){0, 0, 0, 0} : acc[et]);
        if (kb < 12) wf[kb & 1][et] = *(LAS v4u*)(L + L_WD + et * 14336 + ((kb & 1) ? sw1 : sw0) + ((kb + 2) >> 1) * 128);
      }
      SCHED();
    }
    TS(5);
    {
      const u32 sg = hp ? 0x80008000u : 0u;
      const u32 vo = ((q & 1) * 32 + (tw & 1) * 2 + (q >> 1)) * 1024 + (((sp & 1) * 2 + (tw >> 1)) * 16 + l15) * 16;
      const u32 so = WS_AB + ((((xcd >> 2) * 2 + hp) * 4 + j) * 7 * 32 + ((sp & 15) >> 1) * 4) * 1024;
#pragma unroll
      for (int et = 0; et < 7; et += 2) {
        const int e1 = et < 6 ? et + 1 : et;
        auto a = __builtin_amdgcn_permlane16_swap(pk(acc[et][0], acc[et][1]) ^ sg, pk(acc[e1][0], acc[e1][1]) ^ sg, false, false);
        auto c = __builtin_amdgcn_permlane16_swap(pk(acc[et][2], acc[et][3]) ^ sg, pk(acc[e1][2], acc[e1][3]) ^ sg, false, false);
        st16((v4u){a[0], c[0], a[1], c[1]}, rws, (et < 6 || !(q & 1)) ? vo : OOB, so + et * 32768);
      }
    }
    __builtin_amdgcn_s_setprio(0);
  } else {
    const int lc = xcd + 8 * (r >> 2);
    const rsrc_t rm = mk(wmod, D * 6 * D * 2), rc = mk(cv, D * 2);
    if (lc < 42) {
      v4u w[4]; u32 cc[4];
      __builtin_amdgcn_s_setprio(3);
      LDEP(epm);
#pragma unroll
      for (int i = 0; i < 4; i++) {
        const int row = tw * 112 + i * 28 + (lane >> 1);
        w[i] = ld16(rm, lane < 56 ? row * 5376 + (lane & 1) * 16 : OOB, (lc * 4 + j) * 32, 0);
        cc[i] = __builtin_amdgcn_raw_buffer_load_b16(rc, lane < 56 ? row * 2 : OOB, 0, 0);
      }
      WAITLGKM0(); barrier();
      GETEP(epm);
#pragma unroll
      for (int i = 0; i < 4; i++) asm volatile("" : "+v"(cc[i]));
      float a[8];
#pragma unroll
      for (int i = 0; i < 4; i++) {
        const float c = lo(cc[i]), s = rbf(c * sigm(c));
#pragma unroll
        for (int k = 0; k < 4; k++) {
          a[2 * k] = i ? fmaf(s, lo(w[i][k]), a[2 * k]) : s * lo(w[i][k]);
          a[2 * k + 1] = i ? fmaf(s, hi(w[i][k]), a[2 * k + 1]) : s * hi(w[i][k]);
        }
      }
#pragma unroll
      for (int k = 0; k < 4; k++) a[k] = sum32(a[k], a[k + 4]);
#pragma unroll
      for (int k = 0; k < 2; k++) a[k] = sum16(a[k], a[k + 2]);
#pragma unroll
      for (int k = 0; k < 2; k++) { a[k] += dpp<ROR8>(a[k]); a[k] += dpp<ROR4>(a[k]); a[k] += dpp<QP2>(a[k]); }
      if ((lane & 14) == 0) *(LAS f32x2*)(L + L_MPART + tw * 64 + ((lane & 1) * 8 + (lane >> 5) * 4 + ((lane >> 4) & 1) * 2) * 4) = (f32x2){a[0], a[1]};
      if (lds_inc(L, L_CNT + 12) == 3) {
        f32x4 t = *(LAS f32x4*)(L + L_MPART + (lane & 3) * 16);
#pragma unroll
        for (int k = 1; k < 4; k++) t += *(LAS f32x4*)(L + L_MPART + k * 64 + (lane & 3) * 16);
        st16((v4u){pk(t[0], t[1]), pk(t[2], t[3]), (u32)ep, (u32)(ep >> 32)}, rws, lane < 4 ? lane * 16 : OOB, WS_MODG + (lc * 4 + j) * 64);
      }
      __builtin_amdgcn_s_setprio(0);
    } else {
      WAITLGKM0(); barrier();
    }
    TS(1);
  }

  v4u A[2][16];
  LDEP(epw);
  {
    const rsrc_t rw = mk((kq & 1) ? wim : wre, S * S * 2);
    const u32 vo = ((lane >> 2) & 3) * 16384 + (q * 4 + (lane & 3)) * 16;
    const u32 so = (tq * 128 + tw * 32) * 4096 + (kq >> 1) * 2048 + kw * 1024;
    const u32 f0 = (kw ^ (j & 1)) * 65536;
    v4u G[8][4];
#pragma unroll
    for (int g = 0; g < 10; g++) {
      if (g < 8) {
#pragma unroll
        for (int i = 0; i < 4; i++) G[g][i] = ld16(rw, vo + (g & 3) * 256, so + (f0 ^ ((g >> 2) * 65536)) + i * 4096, 0);
      }
      if (g == 1 && comp) {
        WAITVM(8);
        v2u epf = epw; GETEP(epf);
        if (lds_inc(L, L_CNT + 8) == 3)
          __builtin_amdgcn_raw_buffer_store_b64((v2u){(u32)ep, (u32)(ep >> 32)}, rws, lane == 0 ? 0 : OOB,
                                                WS_BF + ((((xcd >> 2) * 2 + hp) * 4 + j) * 16 + (sp & 15)) * 128, SC1);
        TS(6);
      }
      if (g >= 2) tr4(G[g - 2], &A[(g - 2) >> 2][((g - 2) & 3) * 4]);
      SCHED();
    }
  }
  TS(7); TC(31);
  GETEP(epw);
  const u32 lane16 = lane * 16;
  const u32 bsrc = WS_AB + (kq * 4 + j) * 7 * 32768 + tw * 8192;
  if (!comp) {
    const u32 fo = lane < 4 ? WS_BF + ((kq * 4 + j) * 16 + tw * 4 + lane) * 128 : OOB;
    for (int it = 0; it < SPIN; it++) {
      FENCE();
      v2u g = __builtin_amdgcn_raw_buffer_load_b64(rws, fo, 0, SC1);
      if (allok(lane >= 4 || (g.x == (u32)ep && g.y == (u32)(ep >> 32)))) break;
    }
    lds_wait(L, L_CNT + 8, 4);
    TS(2);
#pragma unroll
    for (int n = 0; n < 3; n++) { dma4k(n * 32768 + tw * 8192, lane16, rws, bsrc + n * 32768); dma4k(n * 32768 + tw * 8192 + 4096, lane16, rws, bsrc + n * 32768 + 4096); }
    WAITVM(16);
  }
  barrier();
  TS(8); TC(16);

  const int o_self = kq * 4 + j;
  const u32 cm_vo = (tw * 4 + (kw ^ (j & 1)) * 2 + (l15 >> 3)) * 28672 + q * 64 + (lane & 7) * 8;
  const u32 cm_so = WS_CM + tq * 16 * 28672 + o_self * 7 * 256;
  v4u mg[8], md[4], xr, wrr, gm;
  {
    u32 ba = kw * 16384 + lane16;
    v4u B[4];
#pragma unroll
    for (int k = 0; k < 4; k++) B[k] = *(LAS v4u*)(L + ba + k * 1024);
    f32x4 keep = {0, 0, 0, 0};
#pragma unroll
    for (int n = 0; n < 7; n++) {
      f32x4 a0, a1, pr;
      const u32 bn = (ba + 32768) & 0x1ffff;
#pragma unroll
      for (int k = 0; k < 16; k++) {
        if (k == 12) {
          if (n < 6) TS(18 + 2 * n);
          if (!comp) { if (n < 2) WAITVM(8); else if (n < 5) WAITVM(9); else if (n == 5) WAITVM(1); }
          barrier();
          if (n < 6) TS(19 + 2 * n);
          if (n > 0) pr = *(LAS f32x4*)(L + L_RED + ((n - 1) & 1) * 8192 + (wave ^ 4) * 1024 + lane16);
        }
        a0 = mfma(B[k & 3], A[0][k], k == 0 ? (f32x4){0, 0, 0, 0} : a0);
        a1 = mfma(B[k & 3], A[1][k], k == 0 ? (f32x4){0, 0, 0, 0} : a1);
        B[k & 3] = k < 12 ? *(LAS v4u*)(L + ba + (k + 4) * 1024) : *(LAS v4u*)(L + bn + (k - 12) * 1024);
        if (k == 13) {
          if (n > 0) { pr += keep; __builtin_amdgcn_raw_buffer_store_b64((v2u){pk(pr[0], pr[1]), pk(pr[2], pr[3])}, rws, cm_vo, cm_so + (n - 1) * 256, SC1); }
          if (!comp && n < 4) {
            const u32 sl = ((n + 3) & 3) * 32768 + tw * 8192;
            dma4k(sl, lane16, rws, bsrc + (n + 3) * 32768); dma4k(sl + 4096, lane16, rws, bsrc + (n + 3) * 32768 + 4096);
          }
        }
        SCHED();
      }
      *(LAS f32x4*)(L + L_RED + (n & 1) * 8192 + wave * 1024 + lane16) = a1;
      keep = a0;
      ba = bn;
      if (n == 5) {
        const int row = tq * 128 + o_self * 8 + wave;
        xr = ld16(mk(x, S * D * 2), lane < 56 ? lane16 : OOB, row * 896, 0);
        wrr = ld16(mk(wr, D * 2), lane16, 0, 0);
        gm = ld16(rws, wave < 7 ? lane16 : OOB, WS_MODG + 224 * 16 + wave * 1024, 1);
        if (b < NMLP) {
          const rsrc_t rg = mk(wg, D * F * 2), ru = mk(wu, D * F * 2), rdn = mk(wdn, D * F * 2);
          const u32 vo = wave < 7 ? (lane >> 2) * 4 * 3584 + (lane & 3) * 16 : OOB;
#pragma unroll
          for (int i = 0; i < 4; i++) {
            mg[i] = ld16(rg, vo, (wave * 64 + i) * 3584 + b * 64, 0);
            mg[4 + i] = ld16(ru, vo, (wave * 64 + i) * 3584 + b * 64, 0);
            md[i] = ld16(rdn, lane < 56 ? lane16 : OOB, (b * 32 + wave * 4 + i) * 896, 0);
          }
        }
      }
    }
    TS(9); TC(17);
    WAITLGKM0(); barrier();
    f32x4 pr = *(LAS f32x4*)(L + L_RED + (wave ^ 4) * 1024 + lane16);
    pr += keep;
    __builtin_amdgcn_raw_buffer_store_b64((v2u){pk(pr[0], pr[1]), pk(pr[2], pr[3])}, rws, cm_vo, cm_so + 6 * 256, SC1);
  }
  for (int it = 0; it < SPIN; it++) {
    if (allok(wave == 7 || tagok(gm, ep))) break;
    FENCE(); gm = ld16(rws, wave < 7 ? lane16 : OOB, WS_MODG + 224 * 16 + wave * 1024, 1);
  }
  if (wave < 7) *(LAS v2u*)(L + L_MR + (wave * 64 + lane) * 8) = (v2u){gm.x, gm.y};
  WAITVM(0); WAITLGKM0(); barrier();
  if (wave == 0) {
    __builtin_amdgcn_raw_buffer_store_b64((v2u){(u32)ep, (u32)(ep >> 32)}, rws, lane == 0 ? 0 : OOB, WS_CF + (tq * 16 + o_self) * 128, SC1);
    TS(10);
    const u32 fo = lane < 16 ? WS_CF + (tq * 16 + lane) * 128 : OOB;
    for (int it = 0; it < SPIN; it++) {
      FENCE();
      v2u g = __builtin_amdgcn_raw_buffer_load_b64(rws, fo, 0, SC1);
      if (allok(lane >= 16 || (g.x == (u32)ep && g.y == (u32)(ep >> 32)))) break;
    }
  }
  const v4u vga = *(LAS v4u*)(L + L_MR + (lane < 56 ? lane16 : 0)), vsb = *(LAS v4u*)(L + L_MR + 896 + (lane < 56 ? lane16 : 0));
  const v4u vcb = *(LAS v4u*)(L + L_MR + 1792 + (lane < 56 ? lane16 : 0)), vgb = *(LAS v4u*)(L + L_MR + 2688 + (lane < 56 ? lane16 : 0));
  barrier();
  TS(11);
  if (wave < 7) {
    v4u p[4];
#pragma unroll
    for (int k = 0; k < 4; k++) p[k] = ld16(rws, lane16, WS_CM + (tq * 16 + o_self) * 28672 + k * 7168 + wave * 1024, 1);
    f32x4 y0, y1;
    y0[0] = (lo(p[0].x) + lo(p[1].x)) + (lo(p[2].x) + lo(p[3].x)); y0[1] = (hi(p[0].x) + hi(p[1].x)) + (hi(p[2].x) + hi(p[3].x));
    y0[2] = (lo(p[0].y) + lo(p[1].y)) + (lo(p[2].y) + lo(p[3].y)); y0[3] = (hi(p[0].y) + hi(p[1].y)) + (hi(p[2].y) + hi(p[3].y));
    y1[0] = (lo(p[0].z) + lo(p[1].z)) + (lo(p[2].z) + lo(p[3].z)); y1[1] = (hi(p[0].z) + hi(p[1].z)) + (hi(p[2].z) + hi(p[3].z));
    y1[2] = (lo(p[0].w) + lo(p[1].w)) + (lo(p[2].w) + lo(p[3].w)); y1[3] = (hi(p[0].w) + hi(p[1].w)) + (hi(p[2].w) + hi(p[3].w));
    const u32 yo = L_Y + (lane & 3) * 2 * 1808 + (wave * 16 + (lane >> 2)) * 16;
    *(LAS f32x4*)(L + yo) = y0; *(LAS f32x4*)(L + yo + 1808) = y1;
  }
  WAITLGKM0(); barrier();
  TS(12);
  const bool act = lane < 56;
  float base[8]; u32 h2p[4]; float rsc;
  {
    const f32x4 ya = *(LAS f32x4*)(L + L_Y + wave * 1808 + (act ? lane * 32 : 0)), yb = *(LAS f32x4*)(L + L_Y + wave * 1808 + (act ? lane * 32 : 0) + 16);
    float x1[8];
#pragma unroll
    for (int k = 0; k < 4; k++) {
      const float y0 = k < 2 ? ya[2 * k] : yb[2 * k - 4], y1 = k < 2 ? ya[2 * k + 1] : yb[2 * k - 3];
      x1[2 * k] = fmaf(lo(vga[k]), y0, lo(xr[k])); x1[2 * k + 1] = fmaf(hi(vga[k]), y1, hi(xr[k]));
    }
    float s = ((x1[0] + x1[1]) + (x1[2] + x1[3])) + ((x1[4] + x1[5]) + (x1[6] + x1[7]));
    const float m = wsum(act ? s : 0.f) * (1.f / D);
    float d[8], v = 0;
#pragma unroll
    for (int k = 0; k < 8; k++) { d[k] = x1[k] - m; v = fmaf(d[k], d[k], v); }
    const float rs = __builtin_amdgcn_rsqf(wsum(act ? v : 0.f) * (1.f / D) + 1e-5f);
    float rr = 0;
#pragma unroll
    for (int k = 0; k < 4; k++) {
      const u32 c1 = pk(1.f + lo(vcb[k]), 1.f + hi(vcb[k]));
      h2p[k] = pk(fmaf(d[2 * k] * rs, lo(c1), lo(vsb[k])), fmaf(d[2 * k + 1] * rs, hi(c1), hi(vsb[k])));
      rr = fmaf(lo(h2p[k]), lo(wrr[k]), rr); rr = fmaf(hi(h2p[k]), hi(wrr[k]), rr);
      base[2 * k] = fmaf(lo(vgb[k]), lo(h2p[k]), x1[2 * k]); base[2 * k + 1] = fmaf(hi(vgb[k]), hi(h2p[k]), x1[2 * k + 1]);
    }
    rsc = wsum(act ? rr : 0.f);
  }
  if (lane == 0) *(LAS float*)(L + L_RB + wave * 4) = rsc;
  WAITLGKM0(); barrier();
  const rsrc_t rout = mk(out, S * D * 2);
  const int myrow = tq * 128 + o_self * 8 + wave;
  {
    const f32x4 ra = *(LAS f32x4*)(L + L_RB), rb = *(LAS f32x4*)(L + L_RB + 16);
    int bw = 0; float bv = ra[0];
#pragma unroll
    for (int k = 1; k < 8; k++) { const float c = k < 4 ? ra[k] : rb[k - 4]; if (c > bv) { bv = c; bw = k; } }
    const v4u ov = {pk(base[0], base[1]), pk(base[2], base[3]), pk(base[4], base[5]), pk(base[6], base[7])};
    if (__builtin_amdgcn_readfirstlane(bw) == wave) {
      st16((v4u){fu(rsc), (u32)myrow, (u32)ep, (u32)(ep >> 32)}, rws, lane == 0 ? 0 : OOB, WS_DG + b * 64);
      st16((v4u){h2p[0], h2p[1], (u32)ep, (u32)(ep >> 32)}, rws, act ? lane * 32 : OOB, WS_DH + b * 2048);
      st16((v4u){h2p[2], h2p[3], (u32)ep, (u32)(ep >> 32)}, rws, act ? lane * 32 + 16 : OOB, WS_DH + b * 2048);
      st16((v4u){fu(base[0]), fu(base[1]), fu(base[2]), fu(base[3])}, rws, act ? lane * 32 : OOB, WS_DB + b * 2048);
      st16((v4u){fu(base[4]), fu(base[5]), fu(base[6]), fu(base[7])}, rws, act ? lane * 32 + 16 : OOB, WS_DB + b * 2048);
      st16(ov, rout, act ? lane16 : OOB, myrow * 896);
      WAITVM(0);
      __builtin_amdgcn_raw_buffer_store_b64((v2u){(u32)ep, (u32)(ep >> 32)}, rws, lane == 0 ? 0 : OOB, WS_DF + b * 128, SC1);
    } else {
      st16(ov, rout, act ? lane16 : OOB, myrow * 896);
    }
  }
  TS(13);
  if (b < NMLP) {
    float gw[64], dw[32];
#pragma unroll
    for (int i = 0; i < 8; i++)
#pragma unroll
      for (int k = 0; k < 4; k++) { gw[i * 8 + 2 * k] = lo(mg[i][k]); gw[i * 8 + 2 * k + 1] = hi(mg[i][k]); }
#pragma unroll
    for (int i = 0; i < 4; i++)
#pragma unroll
      for (int k = 0; k < 4; k++) { dw[i * 8 + 2 * k] = lo(md[i][k]); dw[i * 8 + 2 * k + 1] = hi(md[i][k]); }
#pragma unroll
    for (int i = 0; i < 64; i++) asm volatile("" : "+v"(gw[i]));
#pragma unroll
    for (int i = 0; i < 32; i++) asm volatile("" : "+v"(dw[i]));
    {
      v4u g;
      for (int it = 0; it < SPIN; it++) { FENCE(); g = ld16(rws, lane < 32 ? lane * 64 : OOB, WS_DG + wave * 2048, 1); if (allok(lane >= 32 || tagok(g, ep))) break; }
      const float rv = lane < 32 ? uf(g.x) : -__builtin_inff();
      const float mx = wmax(rv);
      const u64 bal = __builtin_amdgcn_ballot_w64(rv == mx && lane < 32);
      const int first = bal ? __builtin_ctzll(bal) : 0;
      if (lane == first) *(LAS v4u*)(L + L_RB2 + wave * 16) = (v4u){g.x, g.y, (u32)(wave * 32 + lane), 0};
    }
    WAITLGKM0(); barrier();
    int win, wrow; float rwin;
    {
      const v4u e = *(LAS v4u*)(L + L_RB2 + (lane & 7) * 16);
      float mx = uf(e.x); mx = fmaxf(mx, dpp<QP1>(mx)); mx = fmaxf(mx, dpp<QP2>(mx)); mx = fmaxf(mx, dpp<ROR4>(mx));
      const u64 bal = __builtin_amdgcn_ballot_w64(uf(e.x) == mx);
      const int first = bal ? __builtin_ctzll(bal) : 0;
      win = __builtin_amdgcn_readlane(e.z, first); wrow = __builtin_amdgcn_readlane(e.y, first); rwin = uf(__builtin_amdgcn_readlane(e.x, first));
    }
    TS(14);
    v4u bs = {0, 0, 0, 0};
    if (wave < 7) {
      v4u g;
      for (int it = 0; it < SPIN; it++) { FENCE(); g = ld16(rws, (lane >> 2) * 16, WS_DH + win * 2048 + wave * 256, 1); if (allok(tagok(g, ep))) break; }
      const float hv[4] = {lo(g.x), hi(g.x), lo(g.y), hi(g.y)};
      float a[16];
#pragma unroll
      for (int k = 0; k < 8; k++) {
        a[k] = fmaf(hv[3], gw[24 + k], fmaf(hv[2], gw[16 + k], fmaf(hv[1], gw[8 + k], hv[0] * gw[k])));
        a[8 + k] = fmaf(hv[3], gw[56 + k], fmaf(hv[2], gw[48 + k], fmaf(hv[1], gw[40 + k], hv[0] * gw[32 + k])));
      }
#pragma unroll
      for (int k = 0; k < 8; k++) a[k] = sum32(a[k], a[k + 8]);
#pragma unroll
      for (int k = 0; k < 4; k++) a[k] = sum16(a[k], a[k + 4]);
#pragma unroll
      for (int k = 0; k < 4; k++) { a[k] += dpp<ROR8>(a[k]); a[k] += dpp<ROR4>(a[k]); }
      if ((lane & 12) == 0) *(LAS f32x4*)(L + L_GU + wave * 256 + (lane >> 5) * 128 + (lane & 3) * 32 + ((lane >> 4) & 1) * 16) = (f32x4){a[0], a[1], a[2], a[3]};
    } else {
      for (int it = 0; it < SPIN; it++) {
        FENCE();
        v2u g = __builtin_amdgcn_raw_buffer_load_b64(rws, 0, WS_DF + win * 128, SC1);
        if (allok(g.x == (u32)ep && g.y == (u32)(ep >> 32))) break;
      }
      FENCE();
      v2u t = __builtin_amdgcn_raw_buffer_load_b64(rws, (lane & 3) * 8, WS_DB + win * 2048 + b * 32, SC1);
      bs.x = t.x; bs.y = t.y;
      bs.z = *(LAS u32*)(L + L_MR + 2688 + b * 16 + (lane & 3) * 4);
    }
    WAITLGKM0(); barrier();
    {
      float v = (lane >> 3) < 7 ? *(LAS float*)(L + L_GU + (lane >> 3) * 256 + ((lane >> 2) & 1) * 128 + wave * 16 + (lane & 3) * 4) : 0.f;
      v += dpp<ROR8>(v); v = sum16(v, v); v = sum32(v, v);
      v = rbf(v);
      const float u = dpp<ROR4>(v);
      const float av = rbf(rbf(v * sigm(v)) * u);
      float y[8];
#pragma unroll
      for (int i = 0; i < 4; i++) {
        const float s = uf(__builtin_amdgcn_readlane(fu(av), i));
#pragma unroll
        for (int k = 0; k < 8; k++) y[k] = i ? fmaf(s, dw[i * 8 + k], y[k]) : s * dw[k];
      }
      if (act) { *(LAS f32x4*)(L + L_DN + wave * 1792 + lane * 32) = (f32x4){y[0], y[1], y[2], y[3]}; *(LAS f32x4*)(L + L_DN + wave * 1792 + lane * 32 + 16) = (f32x4){y[4], y[5], y[6], y[7]}; }
    }
    WAITLGKM0(); barrier();
    if (threadIdx.x < 224) {
      f32x2 t = *(LAS f32x2*)(L + L_DN + threadIdx.x * 8);
#pragma unroll
      for (int k = 1; k < 8; k++) t += *(LAS f32x2*)(L + L_DN + k * 1792 + threadIdx.x * 8);
      st16((v4u){fu(t.x), fu(t.y), (u32)ep, (u32)(ep >> 32)}, rws, threadIdx.x * 16, WS_EG + b * 3584);
    }
    if (wave == 7) {
      const u32 vo = (lane >> 2) * 3584 + (lane & 3) * 16;
      v4u g[4];
      for (int it = 0; it < SPIN; it++) {
        FENCE();
#pragma unroll
        for (int i = 0; i < 4; i++) g[i] = ld16(rws, (i < 3 || lane < 32) ? vo : OOB, WS_EG + i * 16 * 3584 + b * 64, 1);
        if (allok(tagok(g[0], ep) && tagok(g[1], ep) && tagok(g[2], ep) && (lane >= 32 || tagok(g[3], ep)))) break;
      }
      float y0 = (uf(g[0].x) + uf(g[1].x)) + (uf(g[2].x) + uf(g[3].x)), y1 = (uf(g[0].y) + uf(g[1].y)) + (uf(g[2].y) + uf(g[3].y));
      y0 += dpp<ROR4>(y0); y1 += dpp<ROR4>(y1); y0 += dpp<ROR8>(y0); y1 += dpp<ROR8>(y1);
      y0 = sum16(y0, y0); y1 = sum16(y1, y1); y0 = sum32(y0, y0); y1 = sum32(y1, y1);
      const float sg = sigm(rwin);
      const u32 o = pk(fmaf(lo(bs.z), y0 * sg, uf(bs.x)), fmaf(hi(bs.z), y1 * sg, uf(bs.y)));
      __builtin_amdgcn_raw_buffer_store_b32(o, rout, lane < 4 ? lane * 4 : OOB, wrow * 896 + b * 16, SC1);
      if (b == 0) __builtin_amdgcn_raw_buffer_store_b64((v2u){(u32)ep, (u32)(ep >> 32)}, rws, lane == 0 ? 0 : OOB, 0, SC1);
    }
  }
}
}

extern "C" void kernel_launch(void* const* d_in, const int* in_sizes, int n_in, void* d_out, int out_size, void* d_ws, size_t ws_size,
                              hipStream_t stream) {
  (void)hipFuncSetAttribute((const void*)fused_kernel, hipFuncAttributeMaxDynamicSharedMemorySize, L_SIZE);
  fused_kernel<<<256, 512, L_SIZE, stream>>>((const char*)d_in[0], (const char*)d_in[1], (const char*)d_in[2], (const char*)d_in[3],
      (const char*)d_in[4], (const char*)d_in[6], (const char*)d_in[7], (const char*)d_in[8], (const char*)d_in[9], (const char*)d_in[10],
      (const char*)d_in[11], (char*)d_out, (char*)d_ws);
}
```

```cpp
#include <hip/hip_runtime.h>
#include <hip/hip_bf16.h>

typedef unsigned u32;
typedef unsigned long long u64;
typedef u32 v4u __attribute__((ext_vector_type(4)));
typedef u32 v2u __attribute__((ext_vector_type(2)));
typedef __bf16 bfx2 __attribute__((ext_vector_type(2)));
typedef __bf16 bfx8 __attribute__((ext_vector_type(8)));
typedef float f32x4 __attribute__((ext_vector_type(4)));
typedef float f32x2 __attribute__((ext_vector_type(2)));
typedef __amdgpu_buffer_rsrc_t rsrc_t;
#define LAS __attribute__((address_space(3)))
#define DEV __device__ __forceinline__
#define FENCE() asm volatile("" ::: "memory")
#define WAITVM(n) asm volatile("s_waitcnt vmcnt(" #n ")" ::: "memory")
#define WAITLGKM0() asm volatile("s_waitcnt lgkmcnt(0)" ::: "memory")
#define SCHED() __builtin_amdgcn_sched_barrier(0)

namespace {
constexpr int S = 2048, D = 448, F = 1792;
constexpr int SC1 = 16;
constexpr u32 OOB = 0x7ff00000u;
constexpr int SPIN = 1 << 17;
constexpr u32 WS_BF = 16384;
constexpr u32 WS_CF = 49152;
constexpr u32 WS_DF = 81920;
constexpr u32 WS_DG = 114688;
constexpr u32 WS_MODG = 131072;
constexpr u32 WS_DH = 196608;
constexpr u32 WS_DB = WS_DH + 256 * 2048;
constexpr u32 WS_EG = WS_DB + 256 * 2048;
constexpr u32 WS_AB = 2u << 20;
constexpr u32 WS_CM = 6u << 20;
constexpr u32 WS_DBG = 14u << 20;
constexpr u32 WS_SIZE = 16u << 20;
constexpr u32 L_WD = 0, L_X = 100352, L_MP = 157696, L_CNT = 159488, L_RB = 159552, L_RB2 = 159616, L_MPART = 159744;
constexpr u32 L_RED = 131072, L_Y = 131072, L_GU = 0, L_DN = 4096, L_PF = 147456, L_MR = 32768;
constexpr u32 L_SIZE = 163840;
constexpr int NMLP = 56;

DEV rsrc_t mk(const void* p, u32 bytes) { return __builtin_amdgcn_make_buffer_rsrc((void*)p, 0, bytes, 0x00020000); }
DEV v4u ld16(rsrc_t r, u32 v, u32 s, int aux) { return aux ? __builtin_amdgcn_raw_buffer_load_b128(r, v, s, SC1) : __builtin_amdgcn_raw_buffer_load_b128(r, v, s, 0); }
DEV void st16(v4u d, rsrc_t r, u32 v, u32 s) {
  __builtin_amdgcn_raw_buffer_store_b128(d, r, v, s, SC1);
  asm volatile("s_nop 1" :: "v"(d) : "memory");
}
DEV float lo(u32 w) { return __builtin_bit_cast(float, w << 16); }
DEV float hi(u32 w) { return __builtin_bit_cast(float, w & 0xffff0000u); }
DEV u32 pk(float a, float b) { f32x2 v = {a, b}; return __builtin_bit_cast(u32, __builtin_convertvector(v, bfx2)); }
DEV bfx2 bf2(u32 w) { return __builtin_bit_cast(bfx2, w); }
DEV float rbf(float a) { return lo(pk(a, a)); }
DEV u32 fu(float a) { return __builtin_bit_cast(u32, a); }
DEV float uf(u32 a) { return __builtin_bit_cast(float, a); }
template <int C> DEV float dpp(float v) { return uf(__builtin_amdgcn_update_dpp(0u, fu(v), C, 0xf, 0xf, true)); }
constexpr int QP1 = 0xB1, QP2 = 0x4E, ROR4 = 0x124, ROR8 = 0x128;
DEV float sum16(float a, float b) { auto s = __builtin_amdgcn_permlane16_swap(fu(a), fu(b), false, false); return uf(s[0]) + uf(s[1]); }
DEV float sum32(float a, float b) { auto s = __builtin_amdgcn_permlane32_swap(fu(a), fu(b), false, false); return uf(s[0]) + uf(s[1]); }
DEV float max16(float a) { auto s = __builtin_amdgcn_permlane16_swap(fu(a), fu(a), false, false); return fmaxf(uf(s[0]), uf(s[1])); }
DEV float max32(float a) { auto s = __builtin_amdgcn_permlane32_swap(fu(a), fu(a), false, false); return fmaxf(uf(s[0]), uf(s[1])); }
DEV float wsum(float v) { v += dpp<QP1>(v); v += dpp<QP2>(v); v += dpp<ROR4>(v); v += dpp<ROR8>(v); v = sum16(v, v); return sum32(v, v); }
DEV float wmax(float v) { v = fmaxf(v, dpp<QP1>(v)); v = fmaxf(v, dpp<QP2>(v)); v = fmaxf(v, dpp<ROR4>(v)); v = fmaxf(v, dpp<ROR8>(v)); return max32(max16(v)); }
DEV float sigm(float v) { return __builtin_amdgcn_rcpf(1.f + __expf(-v)); }
DEV bool tagok(v4u g, u64 ep) { return g.z == (u32)ep && g.w == (u32)(ep >> 32); }
DEV bool allok(bool p) { return __builtin_amdgcn_ballot_w64(!p) == 0; }
DEV f32x4 mfma(v4u a, v4u b, f32x4 c) { return __builtin_amdgcn_mfma_f32_16x16x32_bf16(__builtin_bit_cast(bfx8, a), __builtin_bit_cast(bfx8, b), c, 0, 0, 0); }
DEV void mma(f32x4& c, v4u a, v4u b) { asm volatile("v_mfma_f32_16x16x32_bf16 %0, %1, %2, %0" : "+v"(c) : "v"(a), "v"(b)); }
DEV void mma0(f32x4& c, v4u a, v4u b) { asm volatile("v_mfma_f32_16x16x32_bf16 %0, %1, %2, 0" : "=v"(c) : "v"(a), "v"(b)); }
DEV float dot2(u32 a, u32 b) { float d; asm volatile("v_dot2_f32_bf16 %0, %1, %2, 0" : "=v"(d) : "v"(a), "v"(b)); return d; }
DEV void barrier() { FENCE(); __builtin_amdgcn_s_barrier(); FENCE(); }

DEV void dma_rows2(u32 m, u32 v, rsrc_t r, u32 s) {
  asm volatile("s_mov_b32 m0, %0\n\ts_nop 0\n\tbuffer_load_dwordx4 %1, %2, %3 offen lds\n\tbuffer_load_dwordx4 %1, %2, %3 offen offset:896 lds"
               :: "s"(m), "v"(v), "s"(r), "s"(s) : "memory");
}
DEV void dma4k(u32 m, u32 v, rsrc_t r, u32 s) {
  asm volatile("s_mov_b32 m0, %0\n\ts_nop 0\n\tbuffer_load_dwordx4 %1, %2, %3 offen sc1 lds\n\tbuffer_load_dwordx4 %1, %2, %3 offen offset:1024 sc1 lds\n\t"
               "buffer_load_dwordx4 %1, %2, %3 offen offset:2048 sc1 lds\n\tbuffer_load_dwordx4 %1, %2, %3 offen offset:3072 sc1 lds"
               :: "s"(m), "v"(v), "s"(r), "s"(s) : "memory");
}
template <int SYS> DEV void dma1k(u32 m, u32 v, rsrc_t r, u32 s) {
  if (SYS) asm volatile("s_mov_b32 m0, %0\n\ts_nop 0\n\tbuffer_load_dwordx4 %1, %2, %3 offen sc1 lds" :: "s"(m), "v"(v), "s"(r), "s"(s) : "memory");
  else asm volatile("s_mov_b32 m0, %0\n\ts_nop 0\n\tbuffer_load_dwordx4 %1, %2, %3 offen lds" :: "s"(m), "v"(v), "s"(r), "s"(s) : "memory");
}
DEV void tr2(u32& a0, u32& a1, u32& b0, u32& b1, u32& c0, u32& c1, u32& d0, u32& d1, u32& t0, u32& t1, u32& u0, u32& u1, u64 m5, u64 m3) {
#define CND(d, x, y, q) "v_cndmask_b32_dpp " d ", " x ", " y ", vcc quad_perm:" q " row_mask:0xf bank_mask:0xf\n\t"
#define Q1 "[1,0,3,2]"
#define Q2 "[2,3,0,1]"
  asm volatile("s_mov_b64 vcc, %12\n\ts_nop 0\n\t"
    CND("%8", "%2", "%0", Q1) CND("%9", "%3", "%1", Q1) CND("%10", "%6", "%4", Q1) CND("%11", "%7", "%5", Q1)
    "s_not_b64 vcc, vcc\n\t"
    CND("%2", "%0", "%2", Q1) CND("%3", "%1", "%3", Q1) CND("%6", "%4", "%6", Q1) CND("%7", "%5", "%7", Q1)
    "s_mov_b64 vcc, %13\n\t"
    CND("%0", "%10", "%8", Q2) CND("%1", "%11", "%9", Q2) CND("%4", "%6", "%2", Q2) CND("%5", "%7", "%3", Q2)
    "s_not_b64 vcc, vcc\n\t"
    CND("%10", "%8", "%10", Q2) CND("%11", "%9", "%11", Q2) CND("%6", "%2", "%6", Q2) CND("%7", "%3", "%7", Q2)
    : "+v"(a0), "+v"(a1), "+v"(b0), "+v"(b1), "+v"(c0), "+v"(c1), "+v"(d0), "+v"(d1), "=&v"(t0), "=&v"(t1), "=&v"(u0), "=&v"(u1)
    : "s"(m5), "s"(m3) : "vcc", "scc");
}
DEV void tr4(const v4u* g, v4u* f) {
  const u64 m5 = 0x5555555555555555ull, m3 = 0x3333333333333333ull;
  u32 a[4] = {g[0].x, g[0].y, g[0].z, g[0].w}, b[4] = {g[1].x, g[1].y, g[1].z, g[1].w};
  u32 c[4] = {g[2].x, g[2].y, g[2].z, g[2].w}, d[4] = {g[3].x, g[3].y, g[3].z, g[3].w}, t[4], u[4];
  tr2(a[0], a[1], b[0], b[1], c[0], c[1], d[0], d[1], t[0], t[1], u[0], u[1], m5, m3);
  tr2(a[2], a[3], b[2], b[3], c[2], c[3], d[2], d[3], t[2], t[3], u[2], u[3], m5, m3);
  f[0] = (v4u){a[0], a[1], a[2], a[3]}; f[1] = (v4u){c[0], c[1], c[2], c[3]};
  f[2] = (v4u){u[0], u[1], u[2], u[3]}; f[3] = (v4u){d[0], d[1], d[2], d[3]};
}
DEV u32 lds_inc(LAS char* L, u32 off) {
  FENCE(); u32 o = 0;
  if ((threadIdx.x & 63) == 0) o = __hip_atomic_fetch_add((LAS u32*)(L + off), 1u, __ATOMIC_RELAXED, __HIP_MEMORY_SCOPE_WORKGROUP);
  FENCE(); return __builtin_amdgcn_readfirstlane(o);
}
DEV void lds_wait(LAS char* L, u32 off, u32 n) {
#pragma unroll 1
  for (int it = 0; it < (1 << 22); it++) { FENCE(); if (*(volatile LAS u32*)(L + off) >= n) break; }
  FENCE();
}
#define TC(i)
#define TS(i)

__global__ void __launch_bounds__(512, 2) fused_kernel(const char* __restrict__ x, const char* __restrict__ wre, const char* __restrict__ wim,
    const char* __restrict__ dre, const char* __restrict__ dim, const char* __restrict__ wr, const char* __restrict__ wg,
    const char* __restrict__ wu, const char* __restrict__ wdn, const char* __restrict__ cv, const char* __restrict__ wmod,
    char* out, char* ws) {
  extern __shared__ __attribute__((aligned(16))) char smem[];
  LAS char* L = (LAS char*)smem;
  TS(0); TC(30);
  const int b = blockIdx.x, xcd = b & 7, r = b >> 3, j = r & 3;
  const int wave = __builtin_amdgcn_readfirstlane(threadIdx.x >> 6), lane = threadIdx.x & 63;
  const int tw = wave & 3, kw = wave >> 2;
  const int kq = xcd & 3, tq = (xcd >> 2) * 8 + (r >> 2);
  const int hp = xcd & 1, sp = (xcd >> 1) * 8 + (r >> 2);
  const bool comp = kw == (j >> 1);
  asm volatile("" :: "s"(x), "s"(wre), "s"(wim), "s"(dre), "s"(dim), "s"(wr), "s"(wg), "s"(wu), "s"(wdn), "s"(cv), "s"(wmod), "s"(out), "s"(ws));
  const rsrc_t rws = mk(ws, WS_SIZE);
#define LDEP(v) FENCE(); v2u v = __builtin_amdgcn_raw_buffer_load_b64(rws, 0, 0, SC1); FENCE()
#define GETEP(v) asm volatile("" : "+v"(v)); const u64 ep = (((u64)(u32)__builtin_amdgcn_readfirstlane(v.y) << 32) | (u32)__builtin_amdgcn_readfirstlane(v.x)) * 6364136223846793005ull + 1442695040888963407ull
  const int q = lane >> 4, l15 = lane & 15;

  if (threadIdx.x < 8) *(LAS u32*)(L + L_CNT + threadIdx.x * 4) = 0;
  WAITLGKM0(); barrier();

  if (comp) {
    __builtin_amdgcn_s_setprio(2);
    {
      const rsrc_t rx = mk(x, S * D * 2), rd = mk(hp ? dim : dre, D * D * 2);
      if (lane < 56) {
        for (int i = 0; i < 8; i++) {
          int p = (i + j * 2) & 7;
          dma_rows2(L_X + tw * 14336 + p * 1792, (lane ^ p) * 16, rx, (sp * 64 + tw * 16 + p * 2) * 896);
        }
      }
      if (lane < 56) {
        const int rot = ((r >> 2) * 7) >> 2;
        for (int i = 0; i < 14; i++) {
          int p = i + rot; if (p >= 14) p -= 14;
          p += tw * 14;
          dma_rows2(L_WD + p * 1792, (lane ^ (p & 7)) * 16, rd, (j * 112 + p * 2) * 896);
        }
      }
    }
    LDEP(epc);
    TS(1);
    WAITVM(29);
    const u32 sw0 = l15 * 896 + ((q ^ (l15 >> 1)) << 4), sw1 = l15 * 896 + (((4 + q) ^ (l15 >> 1)) << 4);
    u32 xn[112];
    {
      v4u t[14];
#pragma unroll
      for (int kb = 0; kb < 14; kb++) t[kb] = *(LAS v4u*)(L + L_X + tw * 14336 + ((kb & 1) ? sw1 : sw0) + (kb >> 1) * 128);
      const v4u ones = {0x3f803f80u, 0x3f803f80u, 0x3f803f80u, 0x3f803f80u};
      f32x4 sm = {0, 0, 0, 0};
#pragma unroll
      for (int kb = 0; kb < 14; kb++) sm = mfma(ones, t[kb], sm);
      const float m = sm[0] * (1.f / D);
      f32x2 v[56], a0 = {0, 0}, a1 = {0, 0};
#pragma unroll
      for (int i = 0; i < 56; i += 2) {
        v[i] = (f32x2){lo(t[i >> 2][i & 3]), hi(t[i >> 2][i & 3])} - m; v[i + 1] = (f32x2){lo(t[i >> 2][(i & 3) + 1]), hi(t[i >> 2][(i & 3) + 1])} - m;
        a0 += v[i] * v[i]; a1 += v[i + 1] * v[i + 1];
      }
      a0 += a1;
      float var = a0.x + a0.y; var = sum16(var, var); var = sum32(var, var);
      const float rs = __builtin_amdgcn_rsqf(var * (1.f / D) + 1e-5f);
#pragma unroll
      for (int i = 0; i < 56; i++) { f32x2 d = v[i] * rs; xn[2 * i] = pk(d.x, 1.f); xn[2 * i + 1] = pk(d.y, 1.f); }
    }
#pragma unroll
    for (int i = 0; i < 112; i++) asm volatile("" : "+v"(xn[i]));
    TS(2);
    WAITVM(0);
    GETEP(epc);
    lds_inc(L, L_CNT + 0);
    lds_wait(L, L_CNT + 0, 4);
    TS(3);
    v4u wf[2][7];
#pragma unroll
    for (int kb = 0; kb < 2; kb++)
#pragma unroll
      for (int et = 0; et < 7; et++) wf[kb][et] = *(LAS v4u*)(L + L_WD + et * 14336 + (kb ? sw1 : sw0));
    {
      const u32 go = lane < 56 ? (tw * 56 + lane) * 16 : OOB;
      v4u g[4];
      _Pragma("unroll 1") for (int it = 0; it < SPIN; it++) {
        FENCE();
#pragma unroll
        for (int p = 0; p < 4; p++) g[p] = ld16(rws, go, WS_MODG + (p & 1) * 21504 + (p >> 1) * 3584, 1);
        if (allok(lane >= 56 || (tagok(g[0], ep) && tagok(g[1], ep) && tagok(g[2], ep) && tagok(g[3], ep)))) break;
      }
      const u32 sh = pk(uf(g[0].x) + uf(g[1].x), uf(g[0].y) + uf(g[1].y)), sc = pk(uf(g[2].x) + uf(g[3].x), uf(g[2].y) + uf(g[3].y));
      const u32 c1 = pk(1.f + lo(sc), 1.f + hi(sc));
      if (lane < 56) *(LAS v2u*)(L + L_MP + (tw * 56 + lane) * 8) = (v2u){(c1 & 0xffffu) | (sh << 16), (c1 >> 16) | (sh & 0xffff0000u)};
    }
    lds_inc(L, L_CNT + 4);
    lds_wait(L, L_CNT + 4, 4);
    TS(4);
    f32x4 acc[7];
    {
      v4u mp[2][2], hf[2];
      float h[8];
#pragma unroll
      for (int k = 0; k < 2; k++) { mp[k][0] = *(LAS v4u*)(L + L_MP + k * 128 + q * 32); mp[k][1] = *(LAS v4u*)(L + L_MP + k * 128 + q * 32 + 16); }
#pragma unroll
      for (int i = 0; i < 8; i++) h[i] = dot2(xn[i], mp[0][i >> 2][i & 3]);
      asm volatile("s_nop 2");
      hf[0] = (v4u){pk(h[0], h[1]), pk(h[2], h[3]), pk(h[4], h[5]), pk(h[6], h[7])};
      asm volatile("s_nop 1" : "+v"(hf[0]));
      SCHED();
#pragma unroll
      for (int kb = 0; kb < 14; kb++) {
        const int c = kb & 1, nx = c ^ 1;
#pragma unroll
        for (int et = 0; et < 7; et++) {
          if (kb == 0) mma0(acc[et], hf[c], wf[c][et]); else mma(acc[et], hf[c], wf[c][et]);
          SCHED();
          if (kb < 13) {
            if (et < 4) { h[2 * et] = dot2(xn[kb * 8 + 8 + 2 * et], mp[nx][et >> 1][(2 * et) & 3]); h[2 * et + 1] = dot2(xn[kb * 8 + 9 + 2 * et], mp[nx][et >> 1][(2 * et + 1) & 3]); }
            if (et == 4) { hf[nx].x = pk(h[0], h[1]); hf[nx].y = pk(h[2], h[3]); }
            if (et == 5) { hf[nx].z = pk(h[4], h[5]); hf[nx].w = pk(h[6], h[7]); }
          }
          SCHED();
          if (kb < 12) {
            wf[c][et] = *(LAS v4u*)(L + L_WD + et * 14336 + (c ? sw1 : sw0) + ((kb + 2) >> 1) * 128);
            if (et < 2) mp[c][et] = *(LAS v4u*)(L + L_MP + (kb + 2) * 128 + q * 32 + et * 16);
          }
          SCHED();
        }
      }
      asm volatile("s_nop 7" : "+v"(acc[0]), "+v"(acc[1]), "+v"(acc[2]), "+v"(acc[3]), "+v"(acc[4]), "+v"(acc[5]), "+v"(acc[6]));
    }
    TS(5);
    {
      const u32 sg = hp ? 0x80008000u : 0u;
      const u32 vo = ((q & 1) * 32 + (tw & 1) * 2 + (q >> 1)) * 1024 + (((sp & 1) * 2 + (tw >> 1)) * 16 + l15) * 16;
      const u32 so = WS_AB + ((((xcd >> 2) * 2 + hp) * 4 + j) * 7 * 32 + ((sp & 15) >> 1) * 4) * 1024;
#pragma unroll
      for (int et = 0; et < 7; et += 2) {
        const int e1 = et < 6 ? et + 1 : et;
        auto a = __builtin_amdgcn_permlane16_swap(pk(acc[et][0], acc[et][1]) ^ sg, pk(acc[e1][0], acc[e1][1]) ^ sg, false, false);
        auto c = __builtin_amdgcn_permlane16_swap(pk(acc[et][2], acc[et][3]) ^ sg, pk(acc[e1][2], acc[e1][3]) ^ sg, false, false);
        st16((v4u){a[0], c[0], a[1], c[1]}, rws, (et < 6 || !(q & 1)) ? vo : OOB, so + et * 32768);
      }
    }
    __builtin_amdgcn_s_setprio(0);
  } else {
    const int mt = (r >> 1) * 8 + xcd;
    if (mt < 84) {
      const rsrc_t rm = mk(wmod, D * 6 * D * 2), rc = mk(cv, D * 2);
      const int ct = (mt >> 1) * 2 + (r & 1);
      v4u w[4]; u32 cc[4];
      __builtin_amdgcn_s_setprio(3);
#pragma unroll
      for (int i = 0; i < 4; i++) {
        const int row = (mt & 1) * 224 + tw * 56 + i * 16 + (lane >> 2);
        const bool on = i < 3 || lane < 32;
        w[i] = ld16(rm, on ? row * 5376 + (lane & 3) * 16 : OOB, ct * 64, 0);
        cc[i] = __builtin_amdgcn_raw_buffer_load_b16(rc, on ? row * 2 : OOB, 0, 0);
      }
      LDEP(epm);
      TS(3);
      float a[8];
#pragma unroll
      for (int i = 0; i < 4; i++) {
        const float c = lo(cc[i]), s = rbf(c * sigm(c));
#pragma unroll
        for (int k = 0; k < 4; k++) {
          a[2 * k] = i ? fmaf(s, lo(w[i][k]), a[2 * k]) : s * lo(w[i][k]);
          a[2 * k + 1] = i ? fmaf(s, hi(w[i][k]), a[2 * k + 1]) : s * hi(w[i][k]);
        }
      }
#pragma unroll
      for (int k = 0; k < 4; k++) a[k] = sum32(a[k], a[k + 4]);
#pragma unroll
      for (int k = 0; k < 2; k++) a[k] = sum16(a[k], a[k + 2]);
#pragma unroll
      for (int k = 0; k < 2; k++) { a[k] += dpp<ROR8>(a[k]); a[k] += dpp<ROR4>(a[k]); }
      if ((lane & 12) == 0) *(LAS f32x2*)(L + L_MPART + tw * 128 + ((lane & 3) * 8 + (lane >> 5) * 4 + ((lane >> 4) & 1) * 2) * 4) = (f32x2){a[0], a[1]};
      TS(6);
      GETEP(epm);
      if (lds_inc(L, L_CNT + 12) == 3) {
        f32x2 t = *(LAS f32x2*)(L + L_MPART + l15 * 8);
#pragma unroll
        for (int k = 1; k < 4; k++) t += *(LAS f32x2*)(L + L_MPART + k * 128 + l15 * 8);
        st16((v4u){fu(t.x), fu(t.y), (u32)ep, (u32)(ep >> 32)}, rws, lane < 16 ? lane * 16 : OOB, WS_MODG + (mt & 1) * 21504 + ct * 256);
      }
      __builtin_amdgcn_s_setprio(0);
    }
    TS(1);
  }

  v4u A[2][16];
  LDEP(epw);
  {
    const rsrc_t rw = mk((kq & 1) ? wim : wre, S * S * 2);
    const u32 vo = ((lane >> 2) & 3) * 16384 + (q * 4 + (lane & 3)) * 16;
    const u32 so = (tq * 128 + tw * 32) * 4096 + (kq >> 1) * 2048 + kw * 1024;
    const u32 f0 = (kw ^ (j & 1)) * 65536;
    v4u G[8][4];
#pragma unroll
    for (int g = 0; g < 10; g++) {
      if (g < 8) {
#pragma unroll
        for (int i = 0; i < 4; i++) G[g][i] = ld16(rw, vo + (g & 3) * 256, so + (f0 ^ ((g >> 2) * 65536)) + i * 4096, 0);
      }
      if (g == 1 && comp) {
        WAITVM(8);
        v2u epf = epw; GETEP(epf);
        if (lds_inc(L, L_CNT + 8) == 3)
          __builtin_amdgcn_raw_buffer_store_b64((v2u){(u32)ep, (u32)(ep >> 32)}, rws, lane == 0 ? 0 : OOB,
                                                WS_BF + ((((xcd >> 2) * 2 + hp) * 4 + j) * 16 + (sp & 15)) * 128, SC1);
        TS(6);
      }
      if (g >= 2) tr4(G[g - 2], &A[(g - 2) >> 2][((g - 2) & 3) * 4]);
      SCHED();
    }
  }
  TS(7); TC(31);
  GETEP(epw);
  const u32 lane16 = lane * 16;
  const u32 bsrc = WS_AB + (kq * 4 + j) * 7 * 32768 + tw * 8192;
  if (!comp) {
    const u32 fo = lane < 4 ? WS_BF + ((kq * 4 + j) * 16 + tw * 4 + lane) * 128 : OOB;
    _Pragma("unroll 1") for (int it = 0; it < SPIN; it++) {
      FENCE();
      v2u g = __builtin_amdgcn_raw_buffer_load_b64(rws, fo, 0, SC1);
      if (allok(lane >= 4 || (g.x == (u32)ep && g.y == (u32)(ep >> 32)))) break;
    }
    lds_wait(L, L_CNT + 8, 4);
    TS(2);
#pragma unroll
    for (int n = 0; n < 3; n++) { dma4k(n * 32768 + tw * 8192, lane16, rws, bsrc + n * 32768); dma4k(n * 32768 + tw * 8192 + 4096, lane16, rws, bsrc + n * 32768 + 4096); }
    WAITVM(16);
  }
  barrier();
  TS(8); TC(16);

  const int o_self = kq * 4 + j;
  const u32 cm_vo = (tw * 4 + (kw ^ (j & 1)) * 2 + (l15 >> 3)) * 28672 + q * 64 + (lane & 7) * 8;
  const u32 cm_so = WS_CM + tq * 16 * 28672 + o_self * 7 * 256;
  v4u xr, wrr, gm[4];
  {
    u32 ba = kw * 16384 + lane16;
    const u32 dsrc = WS_AB + o_self * 7 * 32768 + wave * 4096;
    v4u B[4];
#pragma unroll
    for (int k = 0; k < 4; k++) B[k] = *(LAS v4u*)(L + ba + k * 1024);
    f32x4 a0[2], a1[2], pr;
#pragma unroll
    for (int n = 0; n < 7; n++) {
      const int c = n & 1;
      const u32 bn = (ba + 32768) & 0x1ffff;
#pragma unroll
      for (int k = 0; k < 16; k++) {
        if (k == 12) {
          if (n < 6) TS(18 + 2 * n);
          if (n == 0) WAITVM(8); else if (n == 1) WAITVM(4); else if (n < 4) WAITVM(5); else if (n == 4) WAITVM(8); else if (n == 5) WAITVM(4);
          barrier();
          if (n < 6) TS(19 + 2 * n);
          if (n > 0) pr = *(LAS f32x4*)(L + L_RED + (c ^ 1) * 8192 + (wave ^ 4) * 1024 + lane16);
        }
        if (k == 0) { mma0(a0[c], B[0], A[0][0]); mma0(a1[c], B[0], A[1][0]); }
        else { mma(a0[c], B[k & 3], A[0][k]); mma(a1[c], B[k & 3], A[1][k]); }
        SCHED();
        if (n < 6 || k < 12) B[k & 3] = k < 12 ? *(LAS v4u*)(L + ba + (k + 4) * 1024) : *(LAS v4u*)(L + bn + (k - 12) * 1024);
        if (k == 2 && n > 0) { asm volatile("" : "+v"(a1[c ^ 1])); *(LAS f32x4*)(L + L_RED + (c ^ 1) * 8192 + wave * 1024 + lane16) = a1[c ^ 1]; }
        if (k == 14 && n > 0) { pr += a0[c ^ 1]; __builtin_amdgcn_raw_buffer_store_b64((v2u){pk(pr[0], pr[1]), pk(pr[2], pr[3])}, rws, cm_vo, cm_so + (n - 1) * 256, SC1); }
        if ((k == 13 || k == 15) && n < 4) dma1k<1>(((n + 3) & 3) * 32768 + wave * 4096 + (k - 13) * 512, lane16, rws, dsrc + (n + 3) * 32768 + (k - 13) * 512);
        if ((k == 1 || k == 3) && n >= 1 && n < 5) dma1k<1>(((n + 2) & 3) * 32768 + wave * 4096 + (k + 3) * 512, lane16, rws, dsrc + (n + 2) * 32768 + (k + 3) * 512);
        if (n == 4 && (k == 5 || k == 7)) {
          const int id = (r * 8 + wave) * 2 + (k == 7);
          dma1k<0>(L_PF + wave * 1024, id < 448 ? (lane >> 5) * 3584 + (lane & 31) * 16 : OOB, mk(id >= 224 ? wu : wg, D * F * 2),
                   (id >= 224 ? id - 224 : id) * 7168 + ((xcd * 448) & ~127));
        }
        if (n == 4 && k == 9) dma1k<0>(L_PF + wave * 1024, r * 8 + wave < 196 ? lane16 : OOB, mk(wdn, D * F * 2), xcd * 224 * 896 + (r * 8 + wave) * 1024);
        if (n == 5 && k == 13) {
          xr = ld16(mk(x, S * D * 2), lane < 56 ? lane16 : OOB, (tq * 128 + o_self * 8 + wave) * 896, 0);
          wrr = ld16(mk(wr, D * 2), lane16, 0, 0);
        }
        if (n == 6 && (k & 1) && k < 8) gm[k >> 1] = ld16(rws, wave < 7 ? lane16 : OOB, WS_MODG + ((k >> 1) & 1) * 21504 + (k >> 2) * 1024 + 448 * 16 + wave * 2048, 1);
        SCHED();
      }
      ba = bn;
    }
    TS(9); TC(17);
    asm volatile("s_nop 7" : "+v"(a0[0]), "+v"(a1[0]));
    *(LAS f32x4*)(L + L_RED + wave * 1024 + lane16) = a1[0];
    WAITLGKM0(); barrier();
    pr = *(LAS f32x4*)(L + L_RED + (wave ^ 4) * 1024 + lane16);
    pr += a0[0];
    __builtin_amdgcn_raw_buffer_store_b64((v2u){pk(pr[0], pr[1]), pk(pr[2], pr[3])}, rws, cm_vo, cm_so + 6 * 256, SC1);
  }
  _Pragma("unroll 1") for (int it = 0; it < SPIN; it++) {
    if (allok(wave == 7 || (tagok(gm[0], ep) && tagok(gm[1], ep) && tagok(gm[2], ep) && tagok(gm[3], ep)))) break;
    FENCE();
#pragma unroll
    for (int p = 0; p < 4; p++) gm[p] = ld16(rws, wave < 7 ? lane16 : OOB, WS_MODG + (p & 1) * 21504 + (p >> 1) * 1024 + 448 * 16 + wave * 2048, 1);
  }
  if (wave < 7) {
    const u32 m0 = pk(uf(gm[0].x) + uf(gm[1].x), uf(gm[0].y) + uf(gm[1].y)), m1 = pk(uf(gm[2].x) + uf(gm[3].x), uf(gm[2].y) + uf(gm[3].y));
    *(LAS f32x2*)(L + L_MR + wave * 1024 + lane * 8) = (f32x2){lo(m0), hi(m0)};
    *(LAS f32x2*)(L + L_MR + wave * 1024 + 512 + lane * 8) = (f32x2){lo(m1), hi(m1)};
  }
  WAITVM(0); WAITLGKM0(); barrier();
  if (wave == 0) {
    __builtin_amdgcn_raw_buffer_store_b64((v2u){(u32)ep, (u32)(ep >> 32)}, rws, lane == 0 ? 0 : OOB, WS_CF + (tq * 16 + o_self) * 128, SC1);
    TS(10);
    const u32 fo = lane < 16 ? WS_CF + (tq * 16 + lane) * 128 : OOB;
    _Pragma("unroll 1") for (int it = 0; it < SPIN; it++) {
      FENCE();
      v2u g = __builtin_amdgcn_raw_buffer_load_b64(rws, fo, 0, SC1);
      if (allok(lane >= 16 || (g.x == (u32)ep && g.y == (u32)(ep >> 32)))) break;
    }
  }
  const bool act = lane < 56;
  f32x2 ga[4], sb[4], c1[4], gb[4], xv[4], cw[4];
  float scw, ssw;
  {
    const u32 mo = L_MR + (act ? lane * 32 : 0);
    float p0 = 0, p1 = 0;
#pragma unroll
    for (int k = 0; k < 4; k++) {
      ga[k] = *(LAS f32x2*)(L + mo + k * 8); sb[k] = *(LAS f32x2*)(L + mo + 1792 + k * 8);
      const f32x2 cb = *(LAS f32x2*)(L + mo + 3584 + k * 8); gb[k] = *(LAS f32x2*)(L + mo + 5376 + k * 8);
      const u32 t = pk(1.f + cb.x, 1.f + cb.y);
      c1[k] = (f32x2){lo(t), hi(t)};
      xv[k] = (f32x2){lo(xr[k]), hi(xr[k])};
      const f32x2 w = {lo(wrr[k]), hi(wrr[k])};
      cw[k] = c1[k] * w;
      if (!act) ga[k] = (f32x2){0, 0};
      p0 += cw[k].x + cw[k].y; p1 += sb[k].x * w.x + sb[k].y * w.y;
    }
    scw = wsum(p0); ssw = wsum(p1);
#pragma unroll
    for (int k = 0; k < 4; k++) asm volatile("" : "+v"(ga[k]), "+v"(sb[k]), "+v"(c1[k]), "+v"(gb[k]), "+v"(xv[k]), "+v"(cw[k]));
    asm volatile("" : "+v"(scw), "+v"(ssw));
  }
  barrier();
  TS(11);
  if (wave < 7) {
    v4u p[4];
#pragma unroll
    for (int k = 0; k < 4; k++) p[k] = ld16(rws, lane16, WS_CM + (tq * 16 + o_self) * 28672 + k * 7168 + wave * 1024, 1);
    f32x4 y0, y1;
    y0[0] = (lo(p[0].x) + lo(p[1].x)) + (lo(p[2].x) + lo(p[3].x)); y0[1] = (hi(p[0].x) + hi(p[1].x)) + (hi(p[2].x) + hi(p[3].x));
    y0[2] = (lo(p[0].y) + lo(p[1].y)) + (lo(p[2].y) + lo(p[3].y)); y0[3] = (hi(p[0].y) + hi(p[1].y)) + (hi(p[2].y) + hi(p[3].y));
    y1[0] = (lo(p[0].z) + lo(p[1].z)) + (lo(p[2].z) + lo(p[3].z)); y1[1] = (hi(p[0].z) + hi(p[1].z)) + (hi(p[2].z) + hi(p[3].z));
    y1[2] = (lo(p[0].w) + lo(p[1].w)) + (lo(p[2].w) + lo(p[3].w)); y1[3] = (hi(p[0].w) + hi(p[1].w)) + (hi(p[2].w) + hi(p[3].w));
    const u32 yo = L_Y + (lane & 3) * 2 * 1808 + (wave * 16 + (lane >> 2)) * 16;
    *(LAS f32x4*)(L + yo) = y0; *(LAS f32x4*)(L + yo + 1808) = y1;
  }
  WAITLGKM0(); barrier();
  TS(12);
  const rsrc_t rout = mk(out, S * D * 2);
  const int myrow = tq * 128 + o_self * 8 + wave;
  const int mi = xcd * 7 + r;
  v4u mg[8], md[4];
  {
    const f32x4 ya = *(LAS f32x4*)(L + L_Y + wave * 1808 + (act ? lane * 32 : 0)), yb = *(LAS f32x4*)(L + L_Y + wave * 1808 + (act ? lane * 32 : 0) + 16);
    f32x2 x1[4], d[4];
    x1[0] = ga[0] * (f32x2){ya[0], ya[1]} + xv[0]; x1[1] = ga[1] * (f32x2){ya[2], ya[3]} + xv[1];
    x1[2] = ga[2] * (f32x2){yb[0], yb[1]} + xv[2]; x1[3] = ga[3] * (f32x2){yb[2], yb[3]} + xv[3];
    const float piv = uf(__builtin_amdgcn_readfirstlane(fu(x1[0].x)));
    const float pv = act ? piv : 0.f;
#pragma unroll
    for (int k = 0; k < 4; k++) d[k] = x1[k] - pv;
    const f32x2 t1 = (d[0] + d[1]) + (d[2] + d[3]);
    const f32x2 t2 = d[0] * d[0] + d[1] * d[1] + d[2] * d[2] + d[3] * d[3];
    const f32x2 t3 = d[0] * cw[0] + d[1] * cw[1] + d[2] * cw[2] + d[3] * cw[3];
    float s1 = t1.x + t1.y, s2 = t2.x + t2.y, s3 = t3.x + t3.y;
    s1 += dpp<QP1>(s1); s2 += dpp<QP1>(s2); s3 += dpp<QP1>(s3);
    s1 += dpp<QP2>(s1); s2 += dpp<QP2>(s2); s3 += dpp<QP2>(s3);
    s1 += dpp<ROR4>(s1); s2 += dpp<ROR4>(s2); s3 += dpp<ROR4>(s3);
    s1 += dpp<ROR8>(s1); s2 += dpp<ROR8>(s2); s3 += dpp<ROR8>(s3);
    const float w = sum32(sum16(s1, s2), sum16(s3, s3));
    const float m = uf(__builtin_amdgcn_readlane(fu(w), 0)) * (1.f / D);
    const float var = fmaxf(uf(__builtin_amdgcn_readlane(fu(w), 16)) * (1.f / D) - m * m, 0.f);
    const float rs = __builtin_amdgcn_rsqf(var + 1e-5f);
    const float rsc = rs * (uf(__builtin_amdgcn_readlane(fu(w), 32)) - m * scw) + ssw;
    if (lane == 0) *(LAS float*)(L + L_RB + wave * 4) = rsc;
    WAITLGKM0(); barrier();
    const float rv = *(LAS float*)(L + L_RB + (lane & 7) * 4);
    float mx = fmaxf(rv, dpp<QP1>(rv)); mx = fmaxf(mx, dpp<QP2>(mx)); mx = fmaxf(mx, dpp<ROR4>(mx));
    const u64 bal = __builtin_amdgcn_ballot_w64(rv == mx);
    const bool best = (bal ? __builtin_ctzll(bal) : 0) == wave;
    if (best) st16((v4u){fu(rsc), (u32)myrow, (u32)ep, (u32)(ep >> 32)}, rws, lane == 0 ? 0 : OOB, WS_DG + b * 64);
    u32 h2p[4]; f32x2 base[4];
#pragma unroll
    for (int k = 0; k < 4; k++) {
      const f32x2 h = ((d[k] - m) * rs) * c1[k] + sb[k];
      h2p[k] = pk(h.x, h.y);
      base[k] = gb[k] * (f32x2){lo(h2p[k]), hi(h2p[k])} + x1[k];
    }
    const v4u ov = {pk(base[0].x, base[0].y), pk(base[1].x, base[1].y), pk(base[2].x, base[2].y), pk(base[3].x, base[3].y)};
    if (best) {
      st16((v4u){h2p[0], h2p[1], (u32)ep, (u32)(ep >> 32)}, rws, act ? lane * 32 : OOB, WS_DH + b * 2048);
      st16((v4u){h2p[2], h2p[3], (u32)ep, (u32)(ep >> 32)}, rws, act ? lane * 32 + 16 : OOB, WS_DH + b * 2048);
      st16((v4u){fu(base[0].x), fu(base[0].y), fu(base[1].x), fu(base[1].y)}, rws, act ? lane * 32 : OOB, WS_DB + b * 2048);
      st16((v4u){fu(base[2].x), fu(base[2].y), fu(base[3].x), fu(base[3].y)}, rws, act ? lane * 32 + 16 : OOB, WS_DB + b * 2048);
    }
    st16(ov, rout, act ? lane16 : OOB, myrow * 896);
    if (best) {
      WAITVM(0);
      __builtin_amdgcn_raw_buffer_store_b64((v2u){(u32)ep, (u32)(ep >> 32)}, rws, lane == 0 ? 0 : OOB, WS_DF + b * 128, SC1);
    }
  }
  TS(13);
  if (b < NMLP) {
    {
      const rsrc_t rg = mk(wg, D * F * 2), ru = mk(wu, D * F * 2), rdn = mk(wdn, D * F * 2);
      const u32 vo = wave < 7 ? (lane >> 2) * 4 * 3584 + (lane & 3) * 16 : OOB;
#pragma unroll
      for (int i = 0; i < 4; i++) {
        mg[i] = ld16(rg, vo, (wave * 64 + i) * 3584 + mi * 64, 0);
        mg[4 + i] = ld16(ru, vo, (wave * 64 + i) * 3584 + mi * 64, 0);
      }
#pragma unroll
      for (int i = 0; i < 4; i++) md[i] = ld16(rdn, lane < 56 ? lane16 : OOB, (mi * 32 + wave * 4 + i) * 896, 0);
    }
    float gw[64], dw[32];
#pragma unroll
    for (int i = 0; i < 8; i++)
#pragma unroll
      for (int k = 0; k < 4; k++) { gw[i * 8 + 2 * k] = lo(mg[i][k]); gw[i * 8 + 2 * k + 1] = hi(mg[i][k]); }
#pragma unroll
    for (int i = 0; i < 4; i++)
#pragma unroll
      for (int k = 0; k < 4; k++) { dw[i * 8 + 2 * k] = lo(md[i][k]); dw[i * 8 + 2 * k + 1] = hi(md[i][k]); }
#pragma unroll
    for (int i = 0; i < 64; i++) asm volatile("" : "+v"(gw[i]));
#pragma unroll
    for (int i = 0; i < 32; i++) asm volatile("" : "+v"(dw[i]));
    {
      v4u g;
      _Pragma("unroll 1") for (int it = 0; it < SPIN; it++) { FENCE(); g = ld16(rws, lane < 32 ? lane * 64 : OOB, WS_DG + wave * 2048, 1); if (allok(lane >= 32 || tagok(g, ep))) break; }
      const float rv = lane < 32 ? uf(g.x) : -__builtin_inff();
      const float mx = wmax(rv);
      const u64 bal = __builtin_amdgcn_ballot_w64(rv == mx && lane < 32);
      const int first = bal ? __builtin_ctzll(bal) : 0;
      if (lane == first) *(LAS v4u*)(L + L_RB2 + wave * 16) = (v4u){g.x, g.y, (u32)(wave * 32 + lane), 0};
    }
    WAITLGKM0(); barrier();
    int win, wrow; float rwin;
    {
      const v4u e = *(LAS v4u*)(L + L_RB2 + (lane & 7) * 16);
      float mx = uf(e.x); mx = fmaxf(mx, dpp<QP1>(mx)); mx = fmaxf(mx, dpp<QP2>(mx)); mx = fmaxf(mx, dpp<ROR4>(mx));
      const u64 bal = __builtin_amdgcn_ballot_w64(uf(e.x) == mx);
      const int first = bal ? __builtin_ctzll(bal) : 0;
      win = __builtin_amdgcn_readlane(e.z, first); wrow = __builtin_amdgcn_readlane(e.y, first); rwin = uf(__builtin_amdgcn_readlane(e.x, first));
    }
    TS(14);
    v4u bs = {0, 0, 0, 0};
    if (wave < 7) {
      v4u g;
      _Pragma("unroll 1") for (int it = 0; it < SPIN; it++) { FENCE(); g = ld16(rws, (lane >> 2) * 16, WS_DH + win * 2048 + wave * 256, 1); if (allok(tagok(g, ep))) break; }
      const float hv[4] = {lo(g.x), hi(g.x), lo(g.y), hi(g.y)};
      float a[16];
#pragma unroll
      for (int k = 0; k < 8; k++) {
        a[k] = fmaf(hv[3], gw[24 + k], fmaf(hv[2], gw[16 + k], fmaf(hv[1], gw[8 + k], hv[0] * gw[k])));
        a[8 + k] = fmaf(hv[3], gw[56 + k], fmaf(hv[2], gw[48 + k], fmaf(hv[1], gw[40 + k], hv[0] * gw[32 + k])));
      }
#pragma unroll
      for (int k = 0; k < 8; k++) a[k] = sum32(a[k], a[k + 8]);
#pragma unroll
      for (int k = 0; k < 4; k++) a[k] = sum16(a[k], a[k + 4]);
#pragma unroll
      for (int k = 0; k < 4; k++) { a[k] += dpp<ROR8>(a[k]); a[k] += dpp<ROR4>(a[k]); }
      if ((lane & 12) == 0) *(LAS f32x4*)(L + L_GU + wave * 256 + (lane >> 5) * 128 + (lane & 3) * 32 + ((lane >> 4) & 1) * 16) = (f32x4){a[0], a[1], a[2], a[3]};
    } else {
      _Pragma("unroll 1") for (int it = 0; it < SPIN; it++) {
        FENCE();
        v2u g = __builtin_amdgcn_raw_buffer_load_b64(rws, 0, WS_DF + win * 128, SC1);
        if (allok(g.x == (u32)ep && g.y == (u32)(ep >> 32))) break;
      }
      FENCE();
      v2u t = __builtin_amdgcn_raw_buffer_load_b64(rws, (lane & 3) * 8, WS_DB + win * 2048 + mi * 32, SC1);
      const v2u gbv = *(LAS v2u*)(L + L_MR + 5376 + mi * 32 + (lane & 3) * 8);
      bs = (v4u){t.x, t.y, gbv.x, gbv.y};
    }
    WAITLGKM0(); barrier();
    {
      float v = (lane >> 3) < 7 ? *(LAS float*)(L + L_GU + (lane >> 3) * 256 + ((lane >> 2) & 1) * 128 + wave * 16 + (lane & 3) * 4) : 0.f;
      v += dpp<ROR8>(v); v = sum16(v, v); v = sum32(v, v);
      v = rbf(v);
      const float u = dpp<ROR4>(v);
      const float av = rbf(rbf(v * sigm(v)) * u);
      float y[8];
#pragma unroll
      for (int i = 0; i < 4; i++) {
        const float s = uf(__builtin_amdgcn_readlane(fu(av), i));
#pragma unroll
        for (int k = 0; k < 8; k++) y[k] = i ? fmaf(s, dw[i * 8 + k], y[k]) : s * dw[k];
      }
      if (act) { *(LAS f32x4*)(L + L_DN + wave * 1792 + lane * 32) = (f32x4){y[0], y[1], y[2], y[3]}; *(LAS f32x4*)(L + L_DN + wave * 1792 + lane * 32 + 16) = (f32x4){y[4], y[5], y[6], y[7]}; }
    }
    WAITLGKM0(); barrier();
    if (threadIdx.x < 224) {
      f32x2 t = *(LAS f32x2*)(L + L_DN + threadIdx.x * 8);
#pragma unroll
      for (int k = 1; k < 8; k++) t += *(LAS f32x2*)(L + L_DN + k * 1792 + threadIdx.x * 8);
      st16((v4u){fu(t.x), fu(t.y), (u32)ep, (u32)(ep >> 32)}, rws, threadIdx.x * 16, WS_EG + mi * 3584);
    }
    if (wave == 7) {
      const u32 vo = (lane >> 2) * 3584 + (lane & 3) * 16;
      v4u g[4];
      _Pragma("unroll 1") for (int it = 0; it < SPIN; it++) {
        FENCE();
#pragma unroll
        for (int i = 0; i < 4; i++) g[i] = ld16(rws, (i < 3 || lane < 32) ? vo : OOB, WS_EG + i * 16 * 3584 + mi * 64, 1);
        if (allok(tagok(g[0], ep) && tagok(g[1], ep) && tagok(g[2], ep) && (lane >= 32 || tagok(g[3], ep)))) break;
      }
      float y0 = (uf(g[0].x) + uf(g[1].x)) + (uf(g[2].x) + uf(g[3].x)), y1 = (uf(g[0].y) + uf(g[1].y)) + (uf(g[2].y) + uf(g[3].y));
      y0 += dpp<ROR4>(y0); y1 += dpp<ROR4>(y1); y0 += dpp<ROR8>(y0); y1 += dpp<ROR8>(y1);
      y0 = sum16(y0, y0); y1 = sum16(y1, y1); y0 = sum32(y0, y0); y1 = sum32(y1, y1);
      const float sg = sigm(rwin);
      const u32 o = pk(fmaf(uf(bs.z), y0 * sg, uf(bs.x)), fmaf(uf(bs.w), y1 * sg, uf(bs.y)));
      __builtin_amdgcn_raw_buffer_store_b32(o, rout, lane < 4 ? lane * 4 : OOB, wrow * 896 + mi * 16, SC1);
      if (b == 0) __builtin_amdgcn_raw_buffer_store_b64((v2u){(u32)ep, (u32)(ep >> 32)}, rws, lane == 0 ? 0 : OOB, 0, SC1);
    }
  }
}
}

extern "C" void kernel_launch(void* const* d_in, const int* in_sizes, int n_in, void* d_out, int out_size, void* d_ws, size_t ws_size,
                              hipStream_t stream) {
  (void)hipFuncSetAttribute((const void*)fused_kernel, hipFuncAttributeMaxDynamicSharedMemorySize, L_SIZE);
  fused_kernel<<<256, 512, L_SIZE, stream>>>((const char*)d_in[0], (const char*)d_in[1], (const char*)d_in[2], (const char*)d_in[3],
      (const char*)d_in[4], (const char*)d_in[6], (const char*)d_in[7], (const char*)d_in[8], (const char*)d_in[9], (const char*)d_in[10],
      (const char*)d_in[11], (char*)d_out, (char*)d_ws);
}
```

```cpp
#include <hip/hip_runtime.h>
#include <hip/hip_bf16.h>

typedef unsigned u32;
typedef unsigned long long u64;
typedef u32 v4u __attribute__((ext_vector_type(4)));
typedef u32 v2u __attribute__((ext_vector_type(2)));
typedef __bf16 bfx2 __attribute__((ext_vector_type(2)));
typedef __bf16 bfx8 __attribute__((ext_vector_type(8)));
typedef float f32x4 __attribute__((ext_vector_type(4)));
typedef float f32x2 __attribute__((ext_vector_type(2)));
typedef __amdgpu_buffer_rsrc_t rsrc_t;
#define LAS __attribute__((address_space(3)))
#define DEV __device__ __forceinline__
#define FENCE() asm volatile("" ::: "memory")
#define WAITVM(n) asm volatile("s_waitcnt vmcnt(" #n ")" ::: "memory")
#define WAITLGKM0() asm volatile("s_waitcnt lgkmcnt(0)" ::: "memory")
#define SCHED() __builtin_amdgcn_sched_barrier(0)

namespace {
constexpr int S = 2048, D = 448, F = 1792;
constexpr int SC1 = 16;
constexpr u32 OOB = 0x7ff00000u;
constexpr int SPIN = 1 << 17;
constexpr u32 WS_BF = 16384;
constexpr u32 WS_CF = 49152;
constexpr u32 WS_DF = 81920;
constexpr u32 WS_DG = 114688;
constexpr u32 WS_MODG = 131072;
constexpr u32 WS_DH = 196608;
constexpr u32 WS_DB = WS_DH + 256 * 2048;
constexpr u32 WS_EG = WS_DB + 256 * 2048;
constexpr u32 WS_AB = 2u << 20;
constexpr u32 WS_CM = 6u << 20;
constexpr u32 WS_DBG = 14u << 20;
constexpr u32 WS_SIZE = 16u << 20;
constexpr u32 L_WD = 0, L_X = 100352, L_MP = 157696, L_CNT = 161280, L_RB = 161344, L_RB2 = 161408, L_MPART = 161536;
#define EARLY 1
#define ORD_L 0, 4, 1, 5, 2, 6, 3, 7
constexpr u32 L_RED = 131072, L_Y = 131072, L_GU = 0, L_DN = 4096, L_PF = 147456, L_MR = 32768;
constexpr u32 L_SIZE = 163840;
constexpr int NMLP = 56;

DEV rsrc_t mk(const void* p, u32 bytes) { return __builtin_amdgcn_make_buffer_rsrc((void*)p, 0, bytes, 0x00020000); }
DEV v4u ld16(rsrc_t r, u32 v, u32 s, int aux) { return aux ? __builtin_amdgcn_raw_buffer_load_b128(r, v, s, SC1) : __builtin_amdgcn_raw_buffer_load_b128(r, v, s, 0); }
DEV void st16(v4u d, rsrc_t r, u32 v, u32 s) {
  __builtin_amdgcn_raw_buffer_store_b128(d, r, v, s, SC1);
  asm volatile("s_nop 1" :: "v"(d) : "memory");
}
DEV float lo(u32 w) { return __builtin_bit_cast(float, w << 16); }
DEV float hi(u32 w) { return __builtin_bit_cast(float, w & 0xffff0000u); }
DEV u32 pk(float a, float b) { f32x2 v = {a, b}; return __builtin_bit_cast(u32, __builtin_convertvector(v, bfx2)); }
DEV bfx2 bf2(u32 w) { return __builtin_bit_cast(bfx2, w); }
DEV float rbf(float a) { return lo(pk(a, a)); }
DEV u32 fu(float a) { return __builtin_bit_cast(u32, a); }
DEV float uf(u32 a) { return __builtin_bit_cast(float, a); }
template <int C> DEV float dpp(float v) { return uf(__builtin_amdgcn_update_dpp(0u, fu(v), C, 0xf, 0xf, true)); }
constexpr int QP1 = 0xB1, QP2 = 0x4E, ROR4 = 0x124, ROR8 = 0x128;
DEV float sum16(float a, float b) { auto s = __builtin_amdgcn_permlane16_swap(fu(a), fu(b), false, false); return uf(s[0]) + uf(s[1]); }
DEV float sum32(float a, float b) { auto s = __builtin_amdgcn_permlane32_swap(fu(a), fu(b), false, false); return uf(s[0]) + uf(s[1]); }
DEV float max16(float a) { auto s = __builtin_amdgcn_permlane16_swap(fu(a), fu(a), false, false); return fmaxf(uf(s[0]), uf(s[1])); }
DEV float max32(float a) { auto s = __builtin_amdgcn_permlane32_swap(fu(a), fu(a), false, false); return fmaxf(uf(s[0]), uf(s[1])); }
DEV float wsum(float v) { v += dpp<QP1>(v); v += dpp<QP2>(v); v += dpp<ROR4>(v); v += dpp<ROR8>(v); v = sum16(v, v); return sum32(v, v); }
DEV float wmax(float v) { v = fmaxf(v, dpp<QP1>(v)); v = fmaxf(v, dpp<QP2>(v)); v = fmaxf(v, dpp<ROR4>(v)); v = fmaxf(v, dpp<ROR8>(v)); return max32(max16(v)); }
DEV float sigm(float v) { return __builtin_amdgcn_rcpf(1.f + __expf(-v)); }
DEV u32 bad(v4u g, u64 ep) { return (g.z ^ (u32)ep) | (g.w ^ (u32)(ep >> 32)); }
DEV u32 bad(v2u g, u64 ep) { return (g.x ^ (u32)ep) | (g.y ^ (u32)(ep >> 32)); }
DEV u32 bad4(const v4u* g, u64 ep) { return bad(g[0], ep) | bad(g[1], ep) | bad(g[2], ep) | bad(g[3], ep); }
template <int N> DEV bool clean(u32 b) { return (__builtin_amdgcn_ballot_w64(b != 0) & (N == 64 ? ~0ull : (1ull << (N & 63)) - 1)) == 0; }
DEV f32x4 mfma(v4u a, v4u b, f32x4 c) { return __builtin_amdgcn_mfma_f32_16x16x32_bf16(__builtin_bit_cast(bfx8, a), __builtin_bit_cast(bfx8, b), c, 0, 0, 0); }
DEV void mma(f32x4& c, v4u a, v4u b) { asm volatile("v_mfma_f32_16x16x32_bf16 %0, %1, %2, %0" : "+v"(c) : "v"(a), "v"(b)); }
DEV void mma0(f32x4& c, v4u a, v4u b) { asm volatile("v_mfma_f32_16x16x32_bf16 %0, %1, %2, 0" : "=v"(c) : "v"(a), "v"(b)); }
template <int N> DEV void fmacb(u32& d, float c, float v) { asm volatile("v_fmac_f32_dpp %0, %1, %2 row_newbcast:%3 row_mask:0xf bank_mask:0xf" : "+v"(d) : "v"(c), "v"(v), "n"(N)); }
template <int V> struct IC { static constexpr int value = V; };
template <int N, int I = 0, class Fn> DEV void sfor(Fn&& f) { if constexpr (I < N) { f(IC<I>{}); sfor<N, I + 1>(f); } }
DEV void barrier() { FENCE(); __builtin_amdgcn_s_barrier(); FENCE(); }

template <int SYS> DEV void ld16a(v4u& d, rsrc_t r, u32 v, u32 s) {
  if (SYS) asm volatile("buffer_load_dwordx4 %0, %1, %2, %3 offen sc1" : "=&v"(d) : "v"(v), "s"(r), "s"(s) : "memory");
  else asm volatile("buffer_load_dwordx4 %0, %1, %2, %3 offen" : "=&v"(d) : "v"(v), "s"(r), "s"(s) : "memory");
}
DEV void ld4a(u32& d, rsrc_t r, u32 v, u32 s) { asm volatile("buffer_load_dword %0, %1, %2, %3 offen" : "=&v"(d) : "v"(v), "s"(r), "s"(s) : "memory"); }
DEV void ldepa(v2u& d, rsrc_t r) { asm volatile("buffer_load_dwordx2 %0, off, %1, 0 sc1" : "=&v"(d) : "s"(r) : "memory"); }
#define WAIT7(n, a) asm volatile("s_waitcnt vmcnt(" #n ")" : "+v"((a)[0]), "+v"((a)[1]), "+v"((a)[2]), "+v"((a)[3]), "+v"((a)[4]), "+v"((a)[5]), "+v"((a)[6]) :: "memory")
#define WAIT4(n, a) asm volatile("s_waitcnt vmcnt(" #n ")" : "+v"(a[0]), "+v"(a[1]), "+v"(a[2]), "+v"(a[3]) :: "memory")
DEV void dma_rows2(u32 m, u32 v, rsrc_t r, u32 s) {
  asm volatile("s_mov_b32 m0, %0\n\ts_nop 0\n\tbuffer_load_dwordx4 %1, %2, %3 offen lds\n\tbuffer_load_dwordx4 %1, %2, %3 offen offset:896 lds"
               :: "s"(m), "v"(v), "s"(r), "s"(s) : "memory");
}
DEV void dma4k(u32 m, u32 v, rsrc_t r, u32 s) {
  asm volatile("s_mov_b32 m0, %0\n\ts_nop 0\n\tbuffer_load_dwordx4 %1, %2, %3 offen sc1 lds\n\tbuffer_load_dwordx4 %1, %2, %3 offen offset:1024 sc1 lds\n\t"
               "buffer_load_dwordx4 %1, %2, %3 offen offset:2048 sc1 lds\n\tbuffer_load_dwordx4 %1, %2, %3 offen offset:3072 sc1 lds"
               :: "s"(m), "v"(v), "s"(r), "s"(s) : "memory");
}
template <int SYS> DEV void dma1k(u32 m, u32 v, rsrc_t r, u32 s) {
  if (SYS) asm volatile("s_mov_b32 m0, %0\n\ts_nop 0\n\tbuffer_load_dwordx4 %1, %2, %3 offen sc1 lds" :: "s"(m), "v"(v), "s"(r), "s"(s) : "memory");
  else asm volatile("s_mov_b32 m0, %0\n\ts_nop 0\n\tbuffer_load_dwordx4 %1, %2, %3 offen lds" :: "s"(m), "v"(v), "s"(r), "s"(s) : "memory");
}
DEV void tr2(u32& a0, u32& a1, u32& b0, u32& b1, u32& c0, u32& c1, u32& d0, u32& d1, u32& t0, u32& t1, u32& u0, u32& u1, u64 m5, u64 m3) {
#define CND(d, x, y, q) "v_cndmask_b32_dpp " d ", " x ", " y ", vcc quad_perm:" q " row_mask:0xf bank_mask:0xf\n\t"
#define Q1 "[1,0,3,2]"
#define Q2 "[2,3,0,1]"
  asm volatile("s_mov_b64 vcc, %12\n\ts_nop 0\n\t"
    CND("%8", "%2", "%0", Q1) CND("%9", "%3", "%1", Q1) CND("%10", "%6", "%4", Q1) CND("%11", "%7", "%5", Q1)
    "s_not_b64 vcc, vcc\n\t"
    CND("%2", "%0", "%2", Q1) CND("%3", "%1", "%3", Q1) CND("%6", "%4", "%6", Q1) CND("%7", "%5", "%7", Q1)
    "s_mov_b64 vcc, %13\n\t"
    CND("%0", "%10", "%8", Q2) CND("%1", "%11", "%9", Q2) CND("%4", "%6", "%2", Q2) CND("%5", "%7", "%3", Q2)
    "s_not_b64 vcc, vcc\n\t"
    CND("%10", "%8", "%10", Q2) CND("%11", "%9", "%11", Q2) CND("%6", "%2", "%6", Q2) CND("%7", "%3", "%7", Q2)
    : "+v"(a0), "+v"(a1), "+v"(b0), "+v"(b1), "+v"(c0), "+v"(c1), "+v"(d0), "+v"(d1), "=&v"(t0), "=&v"(t1), "=&v"(u0), "=&v"(u1)
    : "s"(m5), "s"(m3) : "vcc", "scc");
}
DEV void tr4(const v4u* g, v4u* f) {
  const u64 m5 = 0x5555555555555555ull, m3 = 0x3333333333333333ull;
  u32 a[4] = {g[0].x, g[0].y, g[0].z, g[0].w}, b[4] = {g[1].x, g[1].y, g[1].z, g[1].w};
  u32 c[4] = {g[2].x, g[2].y, g[2].z, g[2].w}, d[4] = {g[3].x, g[3].y, g[3].z, g[3].w}, t[4], u[4];
  tr2(a[0], a[1], b[0], b[1], c[0], c[1], d[0], d[1], t[0], t[1], u[0], u[1], m5, m3);
  tr2(a[2], a[3], b[2], b[3], c[2], c[3], d[2], d[3], t[2], t[3], u[2], u[3], m5, m3);
  f[0] = (v4u){a[0], a[1], a[2], a[3]}; f[1] = (v4u){c[0], c[1], c[2], c[3]};
  f[2] = (v4u){u[0], u[1], u[2], u[3]}; f[3] = (v4u){d[0], d[1], d[2], d[3]};
}
DEV void lds_set(LAS char* L, u32 off, u32 v) { FENCE(); *(volatile LAS u32*)(L + off) = v; FENCE(); }
DEV void lds_wait4(LAS char* L, u32 off) {
#pragma unroll 1
  for (int it = 0; it < (1 << 22); it++) {
    FENCE();
    const v4u f = *(volatile LAS v4u*)(L + off);
    if (__builtin_amdgcn_readfirstlane(f.x & f.y & f.z & f.w) & 1) break;
  }
  FENCE();
}
#define TC(i)
#define TS(i)

__global__ void __launch_bounds__(512, 2) fused_kernel(const char* __restrict__ x, const char* __restrict__ wre, const char* __restrict__ wim,
    const char* __restrict__ dre, const char* __restrict__ dim, const char* __restrict__ wr, const char* __restrict__ wg,
    const char* __restrict__ wu, const char* __restrict__ wdn, const char* __restrict__ cv, const char* __restrict__ wmod,
    char* out, char* ws) {
  extern __shared__ __attribute__((aligned(16))) char smem[];
  LAS char* L = (LAS char*)smem;
  TS(0); TC(30);
  const int b = blockIdx.x, xcd = b & 7, r = b >> 3, j = r & 3;
  const int wave = __builtin_amdgcn_readfirstlane(threadIdx.x >> 6), lane = threadIdx.x & 63;
  const int tw = wave & 3, kw = wave >> 2;
  const int kq = xcd & 3, tq = (xcd >> 2) * 8 + (r >> 2);
  const int hp = xcd & 1, sp = (xcd >> 1) * 8 + (r >> 2);
  const bool comp = kw == (j >> 1);
  asm volatile("" :: "s"(x), "s"(wre), "s"(wim), "s"(dre), "s"(dim), "s"(wr), "s"(wg), "s"(wu), "s"(wdn), "s"(cv), "s"(wmod), "s"(out), "s"(ws));
  const rsrc_t rws = mk(ws, WS_SIZE);
  v2u epv; ldepa(epv, rws);
  u64 ep;
#define GETEP() { u32 e0, e1; asm volatile("v_readfirstlane_b32 %0, %2\n\tv_readfirstlane_b32 %1, %3" : "=s"(e0), "=s"(e1) : "v"(epv.x), "v"(epv.y) : "memory"); \
                  ep = (((u64)e1 << 32) | e0) * 6364136223846793005ull + 1442695040888963407ull; }
  const int q = lane >> 4, l15 = lane & 15;

  const rsrc_t rd = mk(hp ? dim : dre, D * D * 2);
  const int mt = (r >> 1) * 8 + xcd, ct = (mt >> 1) * 2 + (r & 1);
  auto mod_load = [&](v4u* mw, u32* mc) {
    const rsrc_t rm = mk(wmod, D * 6 * D * 2), rc = mk(cv, D * 2);
    const u32 row = (mt & 1) * 224 + (lane >> 2) * 2, vo = row * 5376 + (lane & 3) * 16;
#pragma unroll
    for (int i = 0; i < 7; i++) ld4a(mc[i], rc, row * 2, i * 64);
#pragma unroll
    for (int i = 0; i < 14; i++) ld16a<0>(mw[i], rm, vo, ct * 64 + ((i >> 1) * 32 + (i & 1)) * 5376);
  };
  auto mod_silu = [&](u32* mc) {
#pragma unroll
    for (int i = 0; i < 7; i++) { const float c0 = lo(mc[i]), c1 = hi(mc[i]); mc[i] = pk(c0 * sigm(c0), c1 * sigm(c1)); }
  };
  auto mod_finish = [&](v4u* mw, u32* mc) {
    float a[8] = {0, 0, 0, 0, 0, 0, 0, 0};
#pragma unroll
    for (int i = 0; i < 7; i++)
#pragma unroll
      for (int k = 0; k < 4; k++) {
        const u32 w0 = mw[2 * i][k], w1 = mw[2 * i + 1][k];
        a[2 * k] = __builtin_amdgcn_fdot2_f32_bf16(bf2(__builtin_amdgcn_perm(w1, w0, 0x05040100u)), bf2(mc[i]), a[2 * k], false);
        a[2 * k + 1] = __builtin_amdgcn_fdot2_f32_bf16(bf2(__builtin_amdgcn_perm(w1, w0, 0x07060302u)), bf2(mc[i]), a[2 * k + 1], false);
      }
#pragma unroll
    for (int k = 0; k < 4; k++) a[k] = sum32(a[k], a[k + 4]);
#pragma unroll
    for (int k = 0; k < 2; k++) a[k] = sum16(a[k], a[k + 2]);
#pragma unroll
    for (int k = 0; k < 2; k++) { a[k] += dpp<ROR8>(a[k]); a[k] += dpp<ROR4>(a[k]); }
    st16((v4u){fu(a[0]), fu(a[1]), (u32)ep, (u32)(ep >> 32)}, rws, (lane & 12) == 0 ? ((lane & 3) * 4 + (lane >> 5) * 2 + ((lane >> 4) & 1)) * 16 : OOB,
         WS_MODG + (mt & 1) * 21504 + ct * 256);
  };
  const u32 go = lane < 56 ? (tw * 56 + lane) * 16 : OOB;
  auto g_load = [&](v4u* g) {
#pragma unroll
    for (int p = 0; p < 4; p++) ld16a<1>(g[p], rws, go, WS_MODG + (p & 1) * 21504 + (p >> 1) * 3584);
  };
  auto g_put = [&](const v4u* g) {
    const u32 sh = pk(uf(g[0].x) + uf(g[1].x), uf(g[0].y) + uf(g[1].y)), sc = pk(uf(g[2].x) + uf(g[3].x), uf(g[2].y) + uf(g[3].y));
    const u32 c1 = pk(1.f + lo(sc), 1.f + hi(sc));
    if (lane < 56) {
      *(LAS f32x2*)(L + L_MP + (tw * 56 + lane) * 8) = (f32x2){lo(sh), hi(sh)};
      *(LAS f32x2*)(L + L_MP + 1792 + (tw * 56 + lane) * 8) = (f32x2){lo(c1), hi(c1)};
    }
    lds_set(L, L_CNT + 16 + tw * 4, 1);
  };
  if (threadIdx.x < 12) *(LAS u32*)(L + L_CNT + threadIdx.x * 4) = 0;
  WAITLGKM0();
  if (comp) {
    barrier();
    __builtin_amdgcn_s_setprio(2);
    const rsrc_t rx = mk(x, S * D * 2);
    if (lane < 56) {
      for (int i = 0; i < 8; i++) {
        int p = (i + j * 2) & 7;
        dma_rows2(L_X + tw * 14336 + p * 1792, (lane ^ p) * 16, rx, (sp * 64 + tw * 16 + p * 2) * 896);
      }
    }
    TS(1); WAITVM(0); GETEP();
    const u32 sw0 = l15 * 896 + ((q ^ (l15 >> 1)) << 4), sw1 = l15 * 896 + (((4 + q) ^ (l15 >> 1)) << 4);
    float xn[112];
    {
      v4u t[14];
#pragma unroll
      for (int kb = 0; kb < 14; kb++) t[kb] = *(LAS v4u*)(L + L_X + tw * 14336 + ((kb & 1) ? sw1 : sw0) + (kb >> 1) * 128);
      const v4u ones = {0x3f803f80u, 0x3f803f80u, 0x3f803f80u, 0x3f803f80u};
      f32x4 sm = {0, 0, 0, 0};
#pragma unroll
      for (int kb = 0; kb < 14; kb++) sm = mfma(ones, t[kb], sm);
      const float m = sm[0] * (1.f / D);
      f32x2 v[56], a0 = {0, 0}, a1 = {0, 0};
#pragma unroll
      for (int i = 0; i < 56; i += 2) {
        v[i] = (f32x2){lo(t[i >> 2][i & 3]), hi(t[i >> 2][i & 3])} - m; v[i + 1] = (f32x2){lo(t[i >> 2][(i & 3) + 1]), hi(t[i >> 2][(i & 3) + 1])} - m;
        a0 += v[i] * v[i]; a1 += v[i + 1] * v[i + 1];
      }
      a0 += a1;
      float var = a0.x + a0.y; var = sum16(var, var); var = sum32(var, var);
      const float rs = __builtin_amdgcn_rsqf(var * (1.f / D) + 1e-5f);
#pragma unroll
      for (int i = 0; i < 56; i++) { f32x2 d = v[i] * rs; xn[2 * i] = d.x; xn[2 * i + 1] = d.y; }
    }
#pragma unroll
    for (int i = 0; i < 112; i++) asm volatile("" : "+v"(xn[i]));
    TS(2);
    u32 wa[2][2] = {{sw0, sw0 + 57344}, {sw1, sw1 + 57344}}, ma = L_MP + q * 32, ca = L_MP + 1792 + (l15 >> 3) * 128 + q * 32 + (l15 & 7) * 4;
    asm volatile("" : "+v"(wa[0][0]), "+v"(wa[0][1]), "+v"(wa[1][0]), "+v"(wa[1][1]), "+v"(ma), "+v"(ca));
    lds_wait4(L, L_CNT + 16);
    TS(4);
    f32x4 acc[7];
    {
      v4u wf[2][7], mp[2][2], hf[2];
      float cs[7];
#pragma unroll
      for (int k = 0; k < 2; k++) {
        mp[k][0] = *(LAS v4u*)(L + ma + k * 128); mp[k][1] = *(LAS v4u*)(L + ma + k * 128 + 16);
        if (k == 0) { cs[0] = *(LAS float*)(L + ca); cs[1] = *(LAS float*)(L + ca + 256); }
        SCHED();
#pragma unroll
        for (int et = 0; et < 7; et++) wf[k][et] = *(LAS v4u*)(L + wa[k][et >> 2] + (et & 3) * 14336);
        SCHED();
      }
#pragma unroll
      for (int g = 2; g < 7; g++) cs[g] = *(LAS float*)(L + ca + g * 256);
      SCHED();
      u32 h[8];
#define FM(kb, i, m) { h[i] = m[(i) >> 2][(i) & 3]; fmacb<((kb) & 1) * 8 + (i)>(h[i], cs[(kb) >> 1], xn[(kb) * 8 + (i)]); }
      FM(0, 0, mp[0]) FM(0, 1, mp[0]) FM(0, 2, mp[0]) FM(0, 3, mp[0]) FM(0, 4, mp[0]) FM(0, 5, mp[0]) FM(0, 6, mp[0]) FM(0, 7, mp[0])
      hf[0] = (v4u){pk(uf(h[0]), uf(h[1])), pk(uf(h[2]), uf(h[3])), pk(uf(h[4]), uf(h[5])), pk(uf(h[6]), uf(h[7]))};
      asm volatile("s_nop 1" : "+v"(hf[0]));
      SCHED();
      auto step = [&](auto KB) {
        constexpr int kb = KB.value, c = kb & 1, nx = c ^ 1;
#pragma unroll
        for (int et = 0; et < 7; et++) {
          if (kb == 0) mma0(acc[et], hf[c], wf[c][et]); else mma(acc[et], hf[c], wf[c][et]);
          SCHED();
          if (kb < 12) {
            wf[c][et] = *(LAS v4u*)(L + wa[c][et >> 2] + (et & 3) * 14336 + ((kb + 2) >> 1) * 128);
            if (et < 2) mp[c][et] = *(LAS v4u*)(L + ma + (kb + 2) * 128 + et * 16);
          }
          SCHED();
          if constexpr (kb < 13) {
            if (et == 0) { FM(kb + 1, 0, mp[nx]) FM(kb + 1, 1, mp[nx]) }
            if (et == 1) { FM(kb + 1, 2, mp[nx]) FM(kb + 1, 3, mp[nx]) }
            if (et == 2) { FM(kb + 1, 4, mp[nx]) FM(kb + 1, 5, mp[nx]) }
            if (et == 3) { FM(kb + 1, 6, mp[nx]) FM(kb + 1, 7, mp[nx]) }
            if (et == 4) { hf[nx].x = pk(uf(h[0]), uf(h[1])); hf[nx].y = pk(uf(h[2]), uf(h[3])); }
            if (et == 5) { hf[nx].z = pk(uf(h[4]), uf(h[5])); hf[nx].w = pk(uf(h[6]), uf(h[7])); }
          }
          SCHED();
        }
      };
      sfor<14>(step);
      asm volatile("s_nop 7" : "+v"(acc[0]), "+v"(acc[1]), "+v"(acc[2]), "+v"(acc[3]), "+v"(acc[4]), "+v"(acc[5]), "+v"(acc[6]));
    }
    TS(5);
    {
      const u32 sg = hp ? 0x80008000u : 0u;
      const u32 vo = ((q & 1) * 32 + (tw & 1) * 2 + (q >> 1)) * 1024 + (((sp & 1) * 2 + (tw >> 1)) * 16 + l15) * 16;
      const u32 so = WS_AB + ((((xcd >> 2) * 2 + hp) * 4 + j) * 7 * 32 + ((sp & 15) >> 1) * 4) * 1024;
#pragma unroll
      for (int et = 0; et < 7; et += 2) {
        const int e1 = et < 6 ? et + 1 : et;
        auto a = __builtin_amdgcn_permlane16_swap(pk(acc[et][0], acc[et][1]) ^ sg, pk(acc[e1][0], acc[e1][1]) ^ sg, false, false);
        auto c = __builtin_amdgcn_permlane16_swap(pk(acc[et][2], acc[et][3]) ^ sg, pk(acc[e1][2], acc[e1][3]) ^ sg, false, false);
        st16((v4u){a[0], c[0], a[1], c[1]}, rws, (et < 6 || !(q & 1)) ? vo : OOB, so + et * 32768);
      }
    }
  } else {
    int p0 = tw * 14, np = 14;
    if (mt < 28) { p0 = tw ? tw * 17 - 12 : 0; np = tw ? 17 : 5; }
    if (mt < 28 && tw == 0) {
      v4u mw[14]; u32 mc[7];
      __builtin_amdgcn_s_setprio(3);
      mod_load(mw, mc);
      TS(18);
      barrier();
      WAIT7(14, mc);
      GETEP();
      mod_silu(mc);
      WAIT7(7, mw); WAIT7(0, mw + 7);
      mod_finish(mw, mc);
      TS(20);
      __builtin_amdgcn_s_setprio(1);
    } else barrier();
    if (lane < 56) {
      int p = p0 + (((r >> 2) * np) >> 3);
#pragma unroll 1
      for (int i = 0; i < np; i++) {
        dma_rows2(L_WD + p * 1792, (lane ^ (p & 7)) * 16, rd, (j * 112 + p * 2) * 896);
        if (++p == p0 + np) p = p0;
      }
    }
    __builtin_amdgcn_s_setprio(0);
    TS(1);
  }

  v4u A[2][16];
  {
    const rsrc_t rw = mk((kq & 1) ? wim : wre, S * S * 2);
    const u32 vo = ((lane >> 2) & 3) * 16384 + (q * 4 + (lane & 3)) * 16;
    const u32 so = (tq * 128 + tw * 32) * 4096 + (kq >> 1) * 2048 + kw * 1024;
    const u32 f0 = (kw ^ (j & 1)) * 65536;
    v4u G[8][4];
    auto wload = [&](int s) {
      constexpr int OL[8] = {ORD_L}, OC[8] = {0, 4, 1, 5, 2, 6, 3, 7};
      const int g = OL[s];
      (void)OC;
#pragma unroll
      for (int i = 0; i < 4; i++) G[g][i] = ld16(rw, vo + (g & 3) * 256, so + (f0 ^ ((g >> 2) * 65536)) + i * 4096, 0);
      SCHED();
    };
    auto wtr = [&](int s) { constexpr int OL[8] = {ORD_L}; const int g = OL[s]; tr4(G[g], &A[g >> 2][(g & 3) * 4]); SCHED(); };
    const bool late = !comp && tw == 0 && mt >= 28 && mt < 84;
    v4u lw[14]; u32 lc[7];
    v4u gg[4];
    auto gather = [&]() {
      TS(3);
      if (EARLY == 0) WAIT4(0, gg); else if (EARLY == 1) WAIT4(4, gg); else WAIT4(8, gg);
      GETEP();
      if (!clean<56>(bad4(gg, ep))) {
        _Pragma("unroll 1") for (int it = 0; it < SPIN; it++) { g_load(gg); WAIT4(0, gg); if (clean<56>(bad4(gg, ep))) break; }
      }
      g_put(gg);
      TS(4);
    };
    if (!comp) { g_load(gg); if (EARLY == 0) gather(); }
    wload(0);
    if (EARLY == 1) { if (!comp) gather(); }
    wload(1);
    if (EARLY == 2) { if (!comp) gather(); }
    if (comp) {
      WAITVM(8);
      lds_set(L, L_CNT + 32 + tw * 4, 1);
      if (tw == 0) {
        lds_wait4(L, L_CNT + 32);
        __builtin_amdgcn_raw_buffer_store_b64((v2u){(u32)ep, (u32)(ep >> 32)}, rws, lane == 0 ? 0 : OOB,
                                              WS_BF + ((((xcd >> 2) * 2 + hp) * 4 + j) * 16 + (sp & 15)) * 128, SC1);
      }
      TS(6);
    }
    if (late) mod_load(lw, lc);
#pragma unroll
    for (int g = 2; g < 10; g++) { if (g < 8) wload(g); wtr(g - 2); }
    if (late) { WAIT7(0, lc); WAIT7(0, lw); WAIT7(0, lw + 7); mod_silu(lc); mod_finish(lw, lc); }
  }
  __builtin_amdgcn_s_setprio(0);
  TS(7); TC(31);
  const u32 lane16 = lane * 16;
  const u32 bsrc = WS_AB + (kq * 4 + j) * 7 * 32768 + tw * 8192;
  if (!comp) {
    lds_wait4(L, L_CNT + 32);
    const u32 fo = lane < 4 ? WS_BF + ((kq * 4 + j) * 16 + tw * 4 + lane) * 128 : OOB;
    _Pragma("unroll 1") for (int it = 0; it < SPIN; it++) {
      FENCE();
      v2u g = __builtin_amdgcn_raw_buffer_load_b64(rws, fo, 0, SC1);
      if (clean<4>(bad(g, ep))) break;
    }
    TS(2);
#pragma unroll
    for (int n = 0; n < 3; n++) { dma4k(n * 32768 + tw * 8192, lane16, rws, bsrc + n * 32768); dma4k(n * 32768 + tw * 8192 + 4096, lane16, rws, bsrc + n * 32768 + 4096); }
    WAITVM(16);
  }
  barrier();
  TS(8); TC(16);

  const int o_self = kq * 4 + j;
  const u32 cm_vo = (tw * 4 + (kw ^ (j & 1)) * 2 + (l15 >> 3)) * 28672 + q * 64 + (lane & 7) * 8;
  const u32 cm_so = WS_CM + tq * 16 * 28672 + o_self * 7 * 256;
  v4u xr, wrr, gm[4];
  {
    u32 ba = kw * 16384 + lane16;
    const u32 dsrc = WS_AB + o_self * 7 * 32768 + wave * 4096;
    v4u B[4];
#pragma unroll
    for (int k = 0; k < 4; k++) B[k] = *(LAS v4u*)(L + ba + k * 1024);
    f32x4 a0[2], a1[2], pr;
#pragma unroll
    for (int n = 0; n < 7; n++) {
      const int c = n & 1;
      const u32 bn = (ba + 32768) & 0x1ffff;
#pragma unroll
      for (int k = 0; k < 16; k++) {
        if (k == 12) {
          if (n == 0) WAITVM(8); else if (n == 1) WAITVM(4); else if (n < 4) WAITVM(5); else if (n == 4) WAITVM(8); else if (n == 5) WAITVM(4);
          barrier();
          if (n > 0) pr = *(LAS f32x4*)(L + L_RED + (c ^ 1) * 8192 + (wave ^ 4) * 1024 + lane16);
        }
        if (k == 0) { mma0(a0[c], B[0], A[0][0]); mma0(a1[c], B[0], A[1][0]); }
        else { mma(a0[c], B[k & 3], A[0][k]); mma(a1[c], B[k & 3], A[1][k]); }
        SCHED();
        if (n < 6 || k < 12) B[k & 3] = k < 12 ? *(LAS v4u*)(L + ba + (k + 4) * 1024) : *(LAS v4u*)(L + bn + (k - 12) * 1024);
        if (k == 2 && n > 0) { asm volatile("" : "+v"(a1[c ^ 1])); *(LAS f32x4*)(L + L_RED + (c ^ 1) * 8192 + wave * 1024 + lane16) = a1[c ^ 1]; }
        if (k == 14 && n > 0) { pr += a0[c ^ 1]; __builtin_amdgcn_raw_buffer_store_b64((v2u){pk(pr[0], pr[1]), pk(pr[2], pr[3])}, rws, cm_vo, cm_so + (n - 1) * 256, SC1); }
        if ((k == 13 || k == 15) && n < 4) dma1k<1>(((n + 3) & 3) * 32768 + wave * 4096 + (k - 13) * 512, lane16, rws, dsrc + (n + 3) * 32768 + (k - 13) * 512);
        if ((k == 1 || k == 3) && n >= 1 && n < 5) dma1k<1>(((n + 2) & 3) * 32768 + wave * 4096 + (k + 3) * 512, lane16, rws, dsrc + (n + 2) * 32768 + (k + 3) * 512);
        if (n == 4 && (k == 5 || k == 7)) {
          const int id = (r * 8 + wave) * 2 + (k == 7);
          dma1k<0>(L_PF + wave * 1024, id < 448 ? (lane >> 5) * 3584 + (lane & 31) * 16 : OOB, mk(id >= 224 ? wu : wg, D * F * 2),
                   (id >= 224 ? id - 224 : id) * 7168 + ((xcd * 448) & ~127));
        }
        if (n == 4 && k == 9) dma1k<0>(L_PF + wave * 1024, r * 8 + wave < 196 ? lane16 : OOB, mk(wdn, D * F * 2), xcd * 224 * 896 + (r * 8 + wave) * 1024);
        if (n == 5 && k == 13) {
          xr = ld16(mk(x, S * D * 2), lane < 56 ? lane16 : OOB, (tq * 128 + o_self * 8 + wave) * 896, 0);
          wrr = ld16(mk(wr, D * 2), lane16, 0, 0);
        }
        if (n == 6 && (k & 1) && k < 8) gm[k >> 1] = ld16(rws, wave < 7 ? lane16 : OOB, WS_MODG + ((k >> 1) & 1) * 21504 + (k >> 2) * 1024 + 448 * 16 + wave * 2048, 1);
        SCHED();
      }
      ba = bn;
    }
    TS(9); TC(17);
    asm volatile("s_nop 7" : "+v"(a0[0]), "+v"(a1[0]));
    *(LAS f32x4*)(L + L_RED + wave * 1024 + lane16) = a1[0];
    WAITLGKM0(); barrier();
    pr = *(LAS f32x4*)(L + L_RED + (wave ^ 4) * 1024 + lane16);
    pr += a0[0];
    __builtin_amdgcn_raw_buffer_store_b64((v2u){pk(pr[0], pr[1]), pk(pr[2], pr[3])}, rws, cm_vo, cm_so + 6 * 256, SC1);
  }
  _Pragma("unroll 1") for (int it = 0; it < SPIN; it++) {
    if (wave == 7 || clean<64>(bad4(gm, ep))) break;
    FENCE();
#pragma unroll
    for (int p = 0; p < 4; p++) gm[p] = ld16(rws, wave < 7 ? lane16 : OOB, WS_MODG + (p & 1) * 21504 + (p >> 1) * 1024 + 448 * 16 + wave * 2048, 1);
  }
  if (wave < 7) {
    const u32 m0 = pk(uf(gm[0].x) + uf(gm[1].x), uf(gm[0].y) + uf(gm[1].y)), m1 = pk(uf(gm[2].x) + uf(gm[3].x), uf(gm[2].y) + uf(gm[3].y));
    *(LAS f32x2*)(L + L_MR + wave * 1024 + lane * 8) = (f32x2){lo(m0), hi(m0)};
    *(LAS f32x2*)(L + L_MR + wave * 1024 + 512 + lane * 8) = (f32x2){lo(m1), hi(m1)};
  }
  WAITVM(0); WAITLGKM0(); barrier();
  if (wave == 0) {
    __builtin_amdgcn_raw_buffer_store_b64((v2u){(u32)ep, (u32)(ep >> 32)}, rws, lane == 0 ? 0 : OOB, WS_CF + (tq * 16 + o_self) * 128, SC1);
    TS(10);
    const u32 fo = lane < 16 ? WS_CF + (tq * 16 + lane) * 128 : OOB;
    _Pragma("unroll 1") for (int it = 0; it < SPIN; it++) {
      FENCE();
      v2u g = __builtin_amdgcn_raw_buffer_load_b64(rws, fo, 0, SC1);
      if (clean<16>(bad(g, ep))) break;
    }
  }
  const bool act = lane < 56;
  f32x2 ga[4], sb[4], c1[4], gb[4], xv[4], cw[4];
  float scw, ssw;
  {
    const u32 mo = L_MR + (act ? lane * 32 : 0);
    float p0 = 0, p1 = 0;
#pragma unroll
    for (int k = 0; k < 4; k++) {
      ga[k] = *(LAS f32x2*)(L + mo + k * 8); sb[k] = *(LAS f32x2*)(L + mo + 1792 + k * 8);
      const f32x2 cb = *(LAS f32x2*)(L + mo + 3584 + k * 8); gb[k] = *(LAS f32x2*)(L + mo + 5376 + k * 8);
      const u32 t = pk(1.f + cb.x, 1.f + cb.y);
      c1[k] = (f32x2){lo(t), hi(t)};
      xv[k] = (f32x2){lo(xr[k]), hi(xr[k])};
      const f32x2 w = {lo(wrr[k]), hi(wrr[k])};
      cw[k] = c1[k] * w;
      if (!act) ga[k] = (f32x2){0, 0};
      p0 += cw[k].x + cw[k].y; p1 += sb[k].x * w.x + sb[k].y * w.y;
    }
    scw = wsum(p0); ssw = wsum(p1);
#pragma unroll
    for (int k = 0; k < 4; k++) asm volatile("" : "+v"(ga[k]), "+v"(sb[k]), "+v"(c1[k]), "+v"(gb[k]), "+v"(xv[k]), "+v"(cw[k]));
    asm volatile("" : "+v"(scw), "+v"(ssw));
  }
  barrier();
  TS(11);
  if (wave < 7) {
    v4u p[4];
#pragma unroll
    for (int k = 0; k < 4; k++) p[k] = ld16(rws, lane16, WS_CM + (tq * 16 + o_self) * 28672 + k * 7168 + wave * 1024, 1);
    f32x4 y0, y1;
    y0[0] = (lo(p[0].x) + lo(p[1].x)) + (lo(p[2].x) + lo(p[3].x)); y0[1] = (hi(p[0].x) + hi(p[1].x)) + (hi(p[2].x) + hi(p[3].x));
    y0[2] = (lo(p[0].y) + lo(p[1].y)) + (lo(p[2].y) + lo(p[3].y)); y0[3] = (hi(p[0].y) + hi(p[1].y)) + (hi(p[2].y) + hi(p[3].y));
    y1[0] = (lo(p[0].z) + lo(p[1].z)) + (lo(p[2].z) + lo(p[3].z)); y1[1] = (hi(p[0].z) + hi(p[1].z)) + (hi(p[2].z) + hi(p[3].z));
    y1[2] = (lo(p[0].w) + lo(p[1].w)) + (lo(p[2].w) + lo(p[3].w)); y1[3] = (hi(p[0].w) + hi(p[1].w)) + (hi(p[2].w) + hi(p[3].w));
    const u32 yo = L_Y + (lane & 3) * 2 * 1808 + (wave * 16 + (lane >> 2)) * 16;
    *(LAS f32x4*)(L + yo) = y0; *(LAS f32x4*)(L + yo + 1808) = y1;
  }
  WAITLGKM0(); barrier();
  TS(12);
  const rsrc_t rout = mk(out, S * D * 2);
  const int myrow = tq * 128 + o_self * 8 + wave;
  const int mi = xcd * 7 + r;
  v4u mg[8], md[4];
  {
    const f32x4 ya = *(LAS f32x4*)(L + L_Y + wave * 1808 + (act ? lane * 32 : 0)), yb = *(LAS f32x4*)(L + L_Y + wave * 1808 + (act ? lane * 32 : 0) + 16);
    f32x2 x1[4], d[4];
    x1[0] = ga[0] * (f32x2){ya[0], ya[1]} + xv[0]; x1[1] = ga[1] * (f32x2){ya[2], ya[3]} + xv[1];
    x1[2] = ga[2] * (f32x2){yb[0], yb[1]} + xv[2]; x1[3] = ga[3] * (f32x2){yb[2], yb[3]} + xv[3];
    const float piv = uf(__builtin_amdgcn_readfirstlane(fu(x1[0].x)));
    const float pv = act ? piv : 0.f;
#pragma unroll
    for (int k = 0; k < 4; k++) d[k] = x1[k] - pv;
    const f32x2 t1 = (d[0] + d[1]) + (d[2] + d[3]);
    const f32x2 t2 = d[0] * d[0] + d[1] * d[1] + d[2] * d[2] + d[3] * d[3];
    const f32x2 t3 = d[0] * cw[0] + d[1] * cw[1] + d[2] * cw[2] + d[3] * cw[3];
    float s1 = t1.x + t1.y, s2 = t2.x + t2.y, s3 = t3.x + t3.y;
    s1 += dpp<QP1>(s1); s2 += dpp<QP1>(s2); s3 += dpp<QP1>(s3);
    s1 += dpp<QP2>(s1); s2 += dpp<QP2>(s2); s3 += dpp<QP2>(s3);
    s1 += dpp<ROR4>(s1); s2 += dpp<ROR4>(s2); s3 += dpp<ROR4>(s3);
    s1 += dpp<ROR8>(s1); s2 += dpp<ROR8>(s2); s3 += dpp<ROR8>(s3);
    const float w = sum32(sum16(s1, s2), sum16(s3, s3));
    const float m = uf(__builtin_amdgcn_readlane(fu(w), 0)) * (1.f / D);
    const float var = fmaxf(uf(__builtin_amdgcn_readlane(fu(w), 16)) * (1.f / D) - m * m, 0.f);
    const float rs = __builtin_amdgcn_rsqf(var + 1e-5f);
    const float rsc = rs * (uf(__builtin_amdgcn_readlane(fu(w), 32)) - m * scw) + ssw;
    if (lane == 0) *(LAS float*)(L + L_RB + wave * 4) = rsc;
    WAITLGKM0(); barrier();
    const float rv = *(LAS float*)(L + L_RB + (lane & 7) * 4);
    float mx = fmaxf(rv, dpp<QP1>(rv)); mx = fmaxf(mx, dpp<QP2>(mx)); mx = fmaxf(mx, dpp<ROR4>(mx));
    const u64 bal = __builtin_amdgcn_ballot_w64(rv == mx);
    const bool best = (bal ? __builtin_ctzll(bal) : 0) == wave;
    if (best) st16((v4u){fu(rsc), (u32)myrow, (u32)ep, (u32)(ep >> 32)}, rws, lane == 0 ? 0 : OOB, WS_DG + b * 64);
    u32 h2p[4]; f32x2 base[4];
#pragma unroll
    for (int k = 0; k < 4; k++) {
      const f32x2 h = ((d[k] - m) * rs) * c1[k] + sb[k];
      h2p[k] = pk(h.x, h.y);
      base[k] = gb[k] * (f32x2){lo(h2p[k]), hi(h2p[k])} + x1[k];
    }
    const v4u ov = {pk(base[0].x, base[0].y), pk(base[1].x, base[1].y), pk(base[2].x, base[2].y), pk(base[3].x, base[3].y)};
    if (best) {
      st16((v4u){h2p[0], h2p[1], (u32)ep, (u32)(ep >> 32)}, rws, act ? lane * 32 : OOB, WS_DH + b * 2048);
      st16((v4u){h2p[2], h2p[3], (u32)ep, (u32)(ep >> 32)}, rws, act ? lane * 32 + 16 : OOB, WS_DH + b * 2048);
      st16((v4u){fu(base[0].x), fu(base[0].y), fu(base[1].x), fu(base[1].y)}, rws, act ? lane * 32 : OOB, WS_DB + b * 2048);
      st16((v4u){fu(base[2].x), fu(base[2].y), fu(base[3].x), fu(base[3].y)}, rws, act ? lane * 32 + 16 : OOB, WS_DB + b * 2048);
    }
    st16(ov, rout, act ? lane16 : OOB, myrow * 896);
    if (best) {
      WAITVM(0);
      __builtin_amdgcn_raw_buffer_store_b64((v2u){(u32)ep, (u32)(ep >> 32)}, rws, lane == 0 ? 0 : OOB, WS_DF + b * 128, SC1);
    }
  }
  TS(13);
  if (b < NMLP) {
    {
      const rsrc_t rg = mk(wg, D * F * 2), ru = mk(wu, D * F * 2), rdn = mk(wdn, D * F * 2);
      const u32 vo = wave < 7 ? (lane >> 2) * 4 * 3584 + (lane & 3) * 16 : OOB;
#pragma unroll
      for (int i = 0; i < 4; i++) {
        mg[i] = ld16(rg, vo, (wave * 64 + i) * 3584 + mi * 64, 0);
        mg[4 + i] = ld16(ru, vo, (wave * 64 + i) * 3584 + mi * 64, 0);
      }
#pragma unroll
      for (int i = 0; i < 4; i++) md[i] = ld16(rdn, lane < 56 ? lane16 : OOB, (mi * 32 + wave * 4 + i) * 896, 0);
    }
    float gw[64], dw[32];
#pragma unroll
    for (int i = 0; i < 8; i++)
#pragma unroll
      for (int k = 0; k < 4; k++) { gw[i * 8 + 2 * k] = lo(mg[i][k]); gw[i * 8 + 2 * k + 1] = hi(mg[i][k]); }
#pragma unroll
    for (int i = 0; i < 4; i++)
#pragma unroll
      for (int k = 0; k < 4; k++) { dw[i * 8 + 2 * k] = lo(md[i][k]); dw[i * 8 + 2 * k + 1] = hi(md[i][k]); }
#pragma unroll
    for (int i = 0; i < 64; i++) asm volatile("" : "+v"(gw[i]));
#pragma unroll
    for (int i = 0; i < 32; i++) asm volatile("" : "+v"(dw[i]));
    {
      v4u g;
      _Pragma("unroll 1") for (int it = 0; it < SPIN; it++) { FENCE(); g = ld16(rws, lane < 32 ? lane * 64 : OOB, WS_DG + wave * 2048, 1); if (clean<32>(bad(g, ep))) break; }
      const float rv = lane < 32 ? uf(g.x) : -__builtin_inff();
      const float mx = wmax(rv);
      const u64 bal = __builtin_amdgcn_ballot_w64(rv == mx && lane < 32);
      const int first = bal ? __builtin_ctzll(bal) : 0;
      if (lane == first) *(LAS v4u*)(L + L_RB2 + wave * 16) = (v4u){g.x, g.y, (u32)(wave * 32 + lane), 0};
    }
    WAITLGKM0(); barrier();
    int win, wrow; float rwin;
    {
      const v4u e = *(LAS v4u*)(L + L_RB2 + (lane & 7) * 16);
      float mx = uf(e.x); mx = fmaxf(mx, dpp<QP1>(mx)); mx = fmaxf(mx, dpp<QP2>(mx)); mx = fmaxf(mx, dpp<ROR4>(mx));
      const u64 bal = __builtin_amdgcn_ballot_w64(uf(e.x) == mx);
      const int first = bal ? __builtin_ctzll(bal) : 0;
      win = __builtin_amdgcn_readlane(e.z, first); wrow = __builtin_amdgcn_readlane(e.y, first); rwin = uf(__builtin_amdgcn_readlane(e.x, first));
    }
    TS(14);
    v4u bs = {0, 0, 0, 0};
    if (wave < 7) {
      v4u g;
      _Pragma("unroll 1") for (int it = 0; it < SPIN; it++) { FENCE(); g = ld16(rws, (lane >> 2) * 16, WS_DH + win * 2048 + wave * 256, 1); if (clean<64>(bad(g, ep))) break; }
      const float hv[4] = {lo(g.x), hi(g.x), lo(g.y), hi(g.y)};
      float a[16];
#pragma unroll
      for (int k = 0; k < 8; k++) {
        a[k] = fmaf(hv[3], gw[24 + k], fmaf(hv[2], gw[16 + k], fmaf(hv[1], gw[8 + k], hv[0] * gw[k])));
        a[8 + k] = fmaf(hv[3], gw[56 + k], fmaf(hv[2], gw[48 + k], fmaf(hv[1], gw[40 + k], hv[0] * gw[32 + k])));
      }
#pragma unroll
      for (int k = 0; k < 8; k++) a[k] = sum32(a[k], a[k + 8]);
#pragma unroll
      for (int k = 0; k < 4; k++) a[k] = sum16(a[k], a[k + 4]);
#pragma unroll
      for (int k = 0; k < 4; k++) { a[k] += dpp<ROR8>(a[k]); a[k] += dpp<ROR4>(a[k]); }
      if ((lane & 12) == 0) *(LAS f32x4*)(L + L_GU + wave * 256 + (lane >> 5) * 128 + (lane & 3) * 32 + ((lane >> 4) & 1) * 16) = (f32x4){a[0], a[1], a[2], a[3]};
    } else {
      _Pragma("unroll 1") for (int it = 0; it < SPIN; it++) {
        FENCE();
        v2u g = __builtin_amdgcn_raw_buffer_load_b64(rws, 0, WS_DF + win * 128, SC1);
        if (clean<64>(bad(g, ep))) break;
      }
      FENCE();
      v2u t = __builtin_amdgcn_raw_buffer_load_b64(rws, (lane & 3) * 8, WS_DB + win * 2048 + mi * 32, SC1);
      const v2u gbv = *(LAS v2u*)(L + L_MR + 5376 + mi * 32 + (lane & 3) * 8);
      bs = (v4u){t.x, t.y, gbv.x, gbv.y};
    }
    WAITLGKM0(); barrier();
    {
      float v = (lane >> 3) < 7 ? *(LAS float*)(L + L_GU + (lane >> 3) * 256 + ((lane >> 2) & 1) * 128 + wave * 16 + (lane & 3) * 4) : 0.f;
      v += dpp<ROR8>(v); v = sum16(v, v); v = sum32(v, v);
      v = rbf(v);
      const float u = dpp<ROR4>(v);
      const float av = rbf(rbf(v * sigm(v)) * u);
      float y[8];
#pragma unroll
      for (int i = 0; i < 4; i++) {
        const float s = uf(__builtin_amdgcn_readlane(fu(av), i));
#pragma unroll
        for (int k = 0; k < 8; k++) y[k] = i ? fmaf(s, dw[i * 8 + k], y[k]) : s * dw[k];
      }
      if (act) { *(LAS f32x4*)(L + L_DN + wave * 1792 + lane * 32) = (f32x4){y[0], y[1], y[2], y[3]}; *(LAS f32x4*)(L + L_DN + wave * 1792 + lane * 32 + 16) = (f32x4){y[4], y[5], y[6], y[7]}; }
    }
    WAITLGKM0(); barrier();
    if (threadIdx.x < 224) {
      f32x2 t = *(LAS f32x2*)(L + L_DN + threadIdx.x * 8);
#pragma unroll
      for (int k = 1; k < 8; k++) t += *(LAS f32x2*)(L + L_DN + k * 1792 + threadIdx.x * 8);
      st16((v4u){fu(t.x), fu(t.y), (u32)ep, (u32)(ep >> 32)}, rws, threadIdx.x * 16, WS_EG + mi * 3584);
    }
    if (wave == 7) {
      const u32 vo = (lane >> 2) * 3584 + (lane & 3) * 16;
      v4u g[4];
      _Pragma("unroll 1") for (int it = 0; it < SPIN; it++) {
        FENCE();
#pragma unroll
        for (int i = 0; i < 4; i++) g[i] = ld16(rws, (i < 3 || lane < 32) ? vo : OOB, WS_EG + i * 16 * 3584 + mi * 64, 1);
        if (clean<64>(bad(g[0], ep) | bad(g[1], ep) | bad(g[2], ep) | (lane < 32 ? bad(g[3], ep) : 0u))) break;
      }
      float y0 = (uf(g[0].x) + uf(g[1].x)) + (uf(g[2].x) + uf(g[3].x)), y1 = (uf(g[0].y) + uf(g[1].y)) + (uf(g[2].y) + uf(g[3].y));
      y0 += dpp<ROR4>(y0); y1 += dpp<ROR4>(y1); y0 += dpp<ROR8>(y0); y1 += dpp<ROR8>(y1);
      y0 = sum16(y0, y0); y1 = sum16(y1, y1); y0 = sum32(y0, y0); y1 = sum32(y1, y1);
      const float sg = sigm(rwin);
      const u32 o = pk(fmaf(uf(bs.z), y0 * sg, uf(bs.x)), fmaf(uf(bs.w), y1 * sg, uf(bs.y)));
      __builtin_amdgcn_raw_buffer_store_b32(o, rout, lane < 4 ? lane * 4 : OOB, wrow * 896 + mi * 16, SC1);
      if (b == 0) __builtin_amdgcn_raw_buffer_store_b64((v2u){(u32)ep, (u32)(ep >> 32)}, rws, lane == 0 ? 0 : OOB, 0, SC1);
    }
  }
}
}

extern "C" void kernel_launch(void* const* d_in, const int* in_sizes, int n_in, void* d_out, int out_size, void* d_ws, size_t ws_size,
                              hipStream_t stream) {
  (void)hipFuncSetAttribute((const void*)fused_kernel, hipFuncAttributeMaxDynamicSharedMemorySize, L_SIZE);
  fused_kernel<<<256, 512, L_SIZE, stream>>>((const char*)d_in[0], (const char*)d_in[1], (const char*)d_in[2], (const char*)d_in[3],
      (const char*)d_in[4], (const char*)d_in[6], (const char*)d_in[7], (const char*)d_in[8], (const char*)d_in[9], (const char*)d_in[10],
      (const char*)d_in[11], (char*)d_out, (char*)d_ws);
}
```

```cpp
#include <hip/hip_runtime.h>
#include <hip/hip_bf16.h>

typedef unsigned u32;
typedef unsigned long long u64;
typedef u32 v4u __attribute__((ext_vector_type(4)));
typedef u32 v2u __attribute__((ext_vector_type(2)));
typedef __bf16 bfx2 __attribute__((ext_vector_type(2)));
typedef __bf16 bfx8 __attribute__((ext_vector_type(8)));
typedef float f32x4 __attribute__((ext_vector_type(4)));
typedef float f32x2 __attribute__((ext_vector_type(2)));
typedef __amdgpu_buffer_rsrc_t rsrc_t;
#define LAS __attribute__((address_space(3)))
#define DEV __device__ __forceinline__
#define FENCE() asm volatile("" ::: "memory")
#define WAITVM(n) asm volatile("s_waitcnt vmcnt(" #n ")" ::: "memory")
#define WAITLGKM0() asm volatile("s_waitcnt lgkmcnt(0)" ::: "memory")
#define SCHED() __builtin_amdgcn_sched_barrier(0)

namespace {
constexpr int S = 2048, D = 448, F = 1792;
constexpr int SC1 = 16;
constexpr u32 OOB = 0x7ff00000u;
constexpr int SPIN = 1 << 17;
constexpr u32 WS_BF = 16384;
constexpr u32 WS_CF = 49152;
constexpr u32 WS_DF = 81920;
constexpr u32 WS_DG = 114688;
constexpr u32 WS_MODG = 131072;
constexpr u32 WS_MODL = 180224;
constexpr u32 WS_DH = 196608;
constexpr u32 WS_DB = WS_DH + 256 * 2048;
constexpr u32 WS_EG = WS_DB + 256 * 2048;
constexpr u32 WS_AB = 2u << 20;
constexpr u32 WS_CM = 6u << 20;
constexpr u32 WS_DBG = 14u << 20;
constexpr u32 WS_SIZE = 16u << 20;
constexpr u32 L_WD = 0, L_X = 100352, L_MP = 157696, L_CNT = 161280, L_RB = 161344, L_RB2 = 161408, L_MPART = 161536;
#define EARLY 1
#define NPRE 2
#define LA 2
#define LMPOS 7
#define ILV 0
#define CRIT 1
#define NOP_
#define NOW_
#define ROT 1
#define BPF 1
#define ORD_L 0, 4, 1, 5, 2, 6, 3, 7
constexpr u32 L_RED = 131072, L_Y = 131072, L_GU = 0, L_DN = 4096, L_PF = 147456, L_MR = 32768;
constexpr u32 L_SIZE = 163840;
constexpr int NMLP = 56;
#define PFC

DEV rsrc_t mk(const void* p, u32 bytes) { return __builtin_amdgcn_make_buffer_rsrc((void*)p, 0, bytes, 0x00020000); }
DEV v4u ld16(rsrc_t r, u32 v, u32 s, int aux) { return aux ? __builtin_amdgcn_raw_buffer_load_b128(r, v, s, SC1) : __builtin_amdgcn_raw_buffer_load_b128(r, v, s, 0); }
DEV void st16(v4u d, rsrc_t r, u32 v, u32 s) {
  __builtin_amdgcn_raw_buffer_store_b128(d, r, v, s, SC1);
  asm volatile("s_nop 1" :: "v"(d) : "memory");
}
DEV float lo(u32 w) { return __builtin_bit_cast(float, w << 16); }
DEV float hi(u32 w) { return __builtin_bit_cast(float, w & 0xffff0000u); }
DEV u32 pk(float a, float b) { f32x2 v = {a, b}; return __builtin_bit_cast(u32, __builtin_convertvector(v, bfx2)); }
DEV bfx2 bf2(u32 w) { return __builtin_bit_cast(bfx2, w); }
DEV float rbf(float a) { return lo(pk(a, a)); }
DEV u32 fu(float a) { return __builtin_bit_cast(u32, a); }
DEV float uf(u32 a) { return __builtin_bit_cast(float, a); }
template <int C> DEV float dpp(float v) { return uf(__builtin_amdgcn_update_dpp(0u, fu(v), C, 0xf, 0xf, true)); }
constexpr int QP1 = 0xB1, QP2 = 0x4E, ROR4 = 0x124, ROR8 = 0x128;
DEV float sum16(float a, float b) { auto s = __builtin_amdgcn_permlane16_swap(fu(a), fu(b), false, false); return uf(s[0]) + uf(s[1]); }
DEV float sum32(float a, float b) { auto s = __builtin_amdgcn_permlane32_swap(fu(a), fu(b), false, false); return uf(s[0]) + uf(s[1]); }
DEV float max16(float a) { auto s = __builtin_amdgcn_permlane16_swap(fu(a), fu(a), false, false); return fmaxf(uf(s[0]), uf(s[1])); }
DEV float max32(float a) { auto s = __builtin_amdgcn_permlane32_swap(fu(a), fu(a), false, false); return fmaxf(uf(s[0]), uf(s[1])); }
DEV float wsum(float v) { v += dpp<QP1>(v); v += dpp<QP2>(v); v += dpp<ROR4>(v); v += dpp<ROR8>(v); v = sum16(v, v); return sum32(v, v); }
DEV float wmax(float v) { v = fmaxf(v, dpp<QP1>(v)); v = fmaxf(v, dpp<QP2>(v)); v = fmaxf(v, dpp<ROR4>(v)); v = fmaxf(v, dpp<ROR8>(v)); return max32(max16(v)); }
DEV float sigm(float v) { return __builtin_amdgcn_rcpf(1.f + __expf(-v)); }
DEV u32 bad(v4u g, u64 ep) { return (g.z ^ (u32)ep) | (g.w ^ (u32)(ep >> 32)); }
DEV u32 bad(v2u g, u64 ep) { return (g.x ^ (u32)ep) | (g.y ^ (u32)(ep >> 32)); }
DEV u32 bad4(const v4u* g, u64 ep) { return bad(g[0], ep) | bad(g[1], ep) | bad(g[2], ep) | bad(g[3], ep); }
template <int N> DEV bool clean(u32 b) { return (__builtin_amdgcn_ballot_w64(b != 0) & (N == 64 ? ~0ull : (1ull << (N & 63)) - 1)) == 0; }
DEV f32x4 mfma(v4u a, v4u b, f32x4 c) { return __builtin_amdgcn_mfma_f32_16x16x32_bf16(__builtin_bit_cast(bfx8, a), __builtin_bit_cast(bfx8, b), c, 0, 0, 0); }
DEV void mma(f32x4& c, v4u a, v4u b) { asm volatile("v_mfma_f32_16x16x32_bf16 %0, %1, %2, %0" : "+v"(c) : "v"(a), "v"(b)); }
DEV void mma0(f32x4& c, v4u a, v4u b) { asm volatile("v_mfma_f32_16x16x32_bf16 %0, %1, %2, 0" : "=v"(c) : "v"(a), "v"(b)); }
template <int N> DEV void fmacb(u32& d, float c, float v) { asm volatile("v_fmac_f32_dpp %0, %1, %2 row_newbcast:%3 row_mask:0xf bank_mask:0xf" : "+v"(d) : "v"(c), "v"(v), "n"(N)); }
template <int V> struct IC { static constexpr int value = V; };
template <int N, int I = 0, class Fn> DEV void sfor(Fn&& f) { if constexpr (I < N) { f(IC<I>{}); sfor<N, I + 1>(f); } }
DEV void barrier() { FENCE(); __builtin_amdgcn_s_barrier(); FENCE(); }

template <int SYS> DEV void ld16a(v4u& d, rsrc_t r, u32 v, u32 s) {
  if (SYS) asm volatile("buffer_load_dwordx4 %0, %1, %2, %3 offen sc1" : "=&v"(d) : "v"(v), "s"(r), "s"(s) : "memory");
  else asm volatile("buffer_load_dwordx4 %0, %1, %2, %3 offen" : "=&v"(d) : "v"(v), "s"(r), "s"(s) : "memory");
}
DEV void ld4a(u32& d, rsrc_t r, u32 v, u32 s) { asm volatile("buffer_load_dword %0, %1, %2, %3 offen" : "=&v"(d) : "v"(v), "s"(r), "s"(s) : "memory"); }
DEV void ldepa(v2u& d, rsrc_t r) { asm volatile("buffer_load_dwordx2 %0, off, %1, 0 sc1" : "=&v"(d) : "s"(r) : "memory"); }
DEV void ld2a(u32& d, rsrc_t r, u32 v, u32 s) { asm volatile("buffer_load_ushort %0, %1, %2, %3 offen" : "=&v"(d) : "v"(v), "s"(r), "s"(s) : "memory"); }
#define WAIT7(n, a) asm volatile("s_waitcnt vmcnt(" #n ")" : "+v"((a)[0]), "+v"((a)[1]), "+v"((a)[2]), "+v"((a)[3]), "+v"((a)[4]), "+v"((a)[5]), "+v"((a)[6]) :: "memory")
#define WAIT4(n, a) asm volatile("s_waitcnt vmcnt(" #n ")" : "+v"(a[0]), "+v"(a[1]), "+v"(a[2]), "+v"(a[3]) :: "memory")
DEV void dma_rows2(u32 m, u32 v, rsrc_t r, u32 s) {
  asm volatile("s_mov_b32 m0, %0\n\ts_nop 0\n\tbuffer_load_dwordx4 %1, %2, %3 offen lds\n\tbuffer_load_dwordx4 %1, %2, %3 offen offset:896 lds"
               :: "s"(m), "v"(v), "s"(r), "s"(s) : "memory");
}
DEV void dma4k(u32 m, u32 v, rsrc_t r, u32 s) {
  asm volatile("s_mov_b32 m0, %0\n\ts_nop 0\n\tbuffer_load_dwordx4 %1, %2, %3 offen sc1 lds\n\tbuffer_load_dwordx4 %1, %2, %3 offen offset:1024 sc1 lds\n\t"
               "buffer_load_dwordx4 %1, %2, %3 offen offset:2048 sc1 lds\n\tbuffer_load_dwordx4 %1, %2, %3 offen offset:3072 sc1 lds"
               :: "s"(m), "v"(v), "s"(r), "s"(s) : "memory");
}
template <int SYS> DEV void dma1k(u32 m, u32 v, rsrc_t r, u32 s) {
  if (SYS) asm volatile("s_mov_b32 m0, %0\n\ts_nop 0\n\tbuffer_load_dwordx4 %1, %2, %3 offen sc1 lds" :: "s"(m), "v"(v), "s"(r), "s"(s) : "memory");
  else asm volatile("s_mov_b32 m0, %0\n\ts_nop 0\n\tbuffer_load_dwordx4 %1, %2, %3 offen lds" :: "s"(m), "v"(v), "s"(r), "s"(s) : "memory");
}
DEV void tr2(u32& a0, u32& a1, u32& b0, u32& b1, u32& c0, u32& c1, u32& d0, u32& d1, u32& t0, u32& t1, u32& u0, u32& u1, u32 odd1, u32 odd2) {
#define CND(d, x, y, q) "v_cndmask_b32_dpp " d ", " x ", " y ", vcc quad_perm:" q " row_mask:0xf bank_mask:0xf\n\t"
#define MOV(d, x, q) "v_mov_b32_dpp " d ", " x " quad_perm:" q " row_mask:0xf bank_mask:0xf\n\t"
#define SEL(d, m, x) "v_bitop3_b32 " d ", " m ", " d ", " x " bitop3:0xca\n\t"
#define Q1 "[1,0,3,2]"
#define Q2 "[2,3,0,1]"
  u32 x0, x1;
  asm volatile("s_mov_b64 vcc, %16\n\ts_nop 0\n\t"
    CND("%8", "%2", "%0", Q1) MOV("%12", "%0", Q1) CND("%9", "%3", "%1", Q1) MOV("%13", "%1", Q1) SEL("%2", "%14", "%12")
    CND("%10", "%6", "%4", Q1) MOV("%12", "%4", Q1) SEL("%3", "%14", "%13") CND("%11", "%7", "%5", Q1) MOV("%13", "%5", Q1) SEL("%6", "%14", "%12")
    "s_mov_b64 vcc, %17\n\t" SEL("%7", "%14", "%13")
    CND("%0", "%10", "%8", Q2) MOV("%12", "%8", Q2) CND("%1", "%11", "%9", Q2) MOV("%13", "%9", Q2) SEL("%10", "%15", "%12")
    CND("%4", "%6", "%2", Q2) MOV("%12", "%2", Q2) SEL("%11", "%15", "%13") CND("%5", "%7", "%3", Q2) MOV("%13", "%3", Q2) SEL("%6", "%15", "%12") SEL("%7", "%15", "%13")
    : "+v"(a0), "+v"(a1), "+v"(b0), "+v"(b1), "+v"(c0), "+v"(c1), "+v"(d0), "+v"(d1), "=&v"(t0), "=&v"(t1), "=&v"(u0), "=&v"(u1), "=&v"(x0), "=&v"(x1)
    : "v"(odd1), "v"(odd2), "s"(0x5555555555555555ull), "s"(0x3333333333333333ull) : "vcc");
}
DEV void tr4(const v4u* g, v4u* f, u32 odd1, u32 odd2) {
  u32 a[4] = {g[0].x, g[0].y, g[0].z, g[0].w}, b[4] = {g[1].x, g[1].y, g[1].z, g[1].w};
  u32 c[4] = {g[2].x, g[2].y, g[2].z, g[2].w}, d[4] = {g[3].x, g[3].y, g[3].z, g[3].w}, t[4], u[4];
  tr2(a[0], a[1], b[0], b[1], c[0], c[1], d[0], d[1], t[0], t[1], u[0], u[1], odd1, odd2);
  tr2(a[2], a[3], b[2], b[3], c[2], c[3], d[2], d[3], t[2], t[3], u[2], u[3], odd1, odd2);
  f[0] = (v4u){a[0], a[1], a[2], a[3]}; f[1] = (v4u){c[0], c[1], c[2], c[3]};
  f[2] = (v4u){u[0], u[1], u[2], u[3]}; f[3] = (v4u){d[0], d[1], d[2], d[3]};
}
DEV void lds_set(LAS char* L, u32 off, u32 v) { FENCE(); *(volatile LAS u32*)(L + off) = v; FENCE(); }
DEV void lds_wait4(LAS char* L, u32 off) {
#pragma unroll 1
  for (int it = 0; it < (1 << 22); it++) {
    FENCE();
    const v4u f = *(volatile LAS v4u*)(L + off);
    if (__builtin_amdgcn_readfirstlane(f.x & f.y & f.z & f.w) & 1) break;
  }
  FENCE();
}
#define TC(i)
#define TS(i)
#define TN(i)
#define TX(i)

__global__ void __launch_bounds__(512, 2) fused_kernel(const char* __restrict__ x, const char* __restrict__ wre, const char* __restrict__ wim,
    const char* __restrict__ dre, const char* __restrict__ dim, const char* __restrict__ wr, const char* __restrict__ wg,
    const char* __restrict__ wu, const char* __restrict__ wdn, const char* __restrict__ cv, const char* __restrict__ wmod,
    char* out, char* ws) {
  extern __shared__ __attribute__((aligned(16))) char smem[];
  LAS char* L = (LAS char*)smem;
  TS(0); TC(30);
  const int b = blockIdx.x, xcd = b & 7, r = b >> 3, j = r & 3;
  const int wave = __builtin_amdgcn_readfirstlane(threadIdx.x >> 6), lane = threadIdx.x & 63;
  const int tw = wave & 3, kw = wave >> 2;
  const int kq = xcd & 3, tq = (xcd >> 2) * 8 + (r >> 2);
  const int hp = xcd & 1, sp = (xcd >> 1) * 8 + (r >> 2);
  const bool comp = kw == (j >> 1);
  asm volatile("" :: "s"(x), "s"(wre), "s"(wim), "s"(dre), "s"(dim), "s"(wr), "s"(wg), "s"(wu), "s"(wdn), "s"(cv), "s"(wmod), "s"(out), "s"(ws));
  const rsrc_t rws = mk(ws, WS_SIZE);
  v2u epv; ldepa(epv, rws);
  u64 ep;
#define GETEP() { u32 e0, e1; asm volatile("v_readfirstlane_b32 %0, %2\n\tv_readfirstlane_b32 %1, %3" : "=s"(e0), "=s"(e1) : "v"(epv.x), "v"(epv.y) : "memory"); \
                  ep = (((u64)e1 << 32) | e0) * 6364136223846793005ull + 1442695040888963407ull; }
  const int q = lane >> 4, l15 = lane & 15;

  const rsrc_t rd = mk(hp ? dim : dre, D * D * 2);
  const int mt = xcd * 7 + (r >> 2), mh = mt >= 28, ct = mt - mh * 28;
  const bool crit = r < 28 && j == ((r >> 2) & 3);
  auto mod_load = [&](v4u* mw, u32* mc) {
    const rsrc_t rm = mk(wmod, D * 6 * D * 2), rc = mk(cv, D * 2);
    const u32 row = mh * 224 + (lane >> 2) * 2, vo = row * 5376 + (lane & 3) * 16;
#pragma unroll
    for (int i = 0; i < 7; i++) ld4a(mc[i], rc, row * 2, i * 64);
#pragma unroll
    for (int i = 0; i < 14; i++) ld16a<0>(mw[i], rm, vo, ct * 64 + ((i >> 1) * 32 + (i & 1)) * 5376);
  };
  auto mod_silu = [&](u32* mc) {
#pragma unroll
    for (int i = 0; i < 7; i++) { const float c0 = lo(mc[i]), c1 = hi(mc[i]); mc[i] = pk(c0 * sigm(c0), c1 * sigm(c1)); }
  };
  auto mod_finish = [&](v4u* mw, u32* mc) {
    float a[8] = {0, 0, 0, 0, 0, 0, 0, 0};
#pragma unroll
    for (int i = 0; i < 7; i++)
#pragma unroll
      for (int k = 0; k < 4; k++) {
        const u32 w0 = mw[2 * i][k], w1 = mw[2 * i + 1][k];
        a[2 * k] = __builtin_amdgcn_fdot2_f32_bf16(bf2(__builtin_amdgcn_perm(w1, w0, 0x05040100u)), bf2(mc[i]), a[2 * k], false);
        a[2 * k + 1] = __builtin_amdgcn_fdot2_f32_bf16(bf2(__builtin_amdgcn_perm(w1, w0, 0x07060302u)), bf2(mc[i]), a[2 * k + 1], false);
      }
#pragma unroll
    for (int k = 0; k < 4; k++) a[k] = sum32(a[k], a[k + 4]);
#pragma unroll
    for (int k = 0; k < 2; k++) a[k] = sum16(a[k], a[k + 2]);
#pragma unroll
    for (int k = 0; k < 2; k++) { a[k] += dpp<ROR8>(a[k]); a[k] += dpp<ROR4>(a[k]); }
    st16((v4u){fu(a[0]), fu(a[1]), (u32)ep, (u32)(ep >> 32)}, rws, (lane & 12) == 0 ? ((lane & 3) * 4 + (lane >> 5) * 2 + ((lane >> 4) & 1)) * 16 : OOB,
         WS_MODG + mh * 21504 + ct * 256);
  };
  const u32 go = lane < 56 ? (tw * 56 + lane) * 16 : OOB;
  auto g_load = [&](v4u* g) {
#pragma unroll
    for (int p = 0; p < 4; p++) ld16a<1>(g[p], rws, go, WS_MODG + (p & 1) * 21504 + (p >> 1) * 3584);
  };
  auto g_put = [&](const v4u* g) {
    const u32 sh = pk(uf(g[0].x) + uf(g[1].x), uf(g[0].y) + uf(g[1].y)), sc = pk(uf(g[2].x) + uf(g[3].x), uf(g[2].y) + uf(g[3].y));
    const u32 c1 = pk(1.f + lo(sc), 1.f + hi(sc));
    if (lane < 56) {
      *(LAS f32x2*)(L + L_MP + (tw * 56 + lane) * 8) = (f32x2){lo(sh), hi(sh)};
      *(LAS f32x2*)(L + L_MP + 1792 + (tw * 56 + lane) * 8) = (f32x2){lo(c1), hi(c1)};
    }
    lds_set(L, L_CNT + 16 + tw * 4, 1);
  };
  if (threadIdx.x < 12) *(LAS u32*)(L + L_CNT + threadIdx.x * 4) = 0;
  WAITLGKM0();
  if (comp) {
    barrier();
    __builtin_amdgcn_s_setprio(2);
    const rsrc_t rx = mk(x, S * D * 2);
    if (lane < 56) {
      for (int i = 0; i < 8; i++) {
        int p = (i + j * 2) & 7;
        dma_rows2(L_X + tw * 14336 + p * 1792, (lane ^ p) * 16, rx, (sp * 64 + tw * 16 + p * 2) * 896);
      }
    }
    TX(1); WAITVM(0); GETEP();
    const u32 sw0 = l15 * 896 + ((q ^ (l15 >> 1)) << 4), sw1 = l15 * 896 + (((4 + q) ^ (l15 >> 1)) << 4);
    float xn[112];
    {
      v4u t[14];
#pragma unroll
      for (int kb = 0; kb < 14; kb++) t[kb] = *(LAS v4u*)(L + L_X + tw * 14336 + ((kb & 1) ? sw1 : sw0) + (kb >> 1) * 128);
      const v4u ones = {0x3f803f80u, 0x3f803f80u, 0x3f803f80u, 0x3f803f80u};
      f32x4 sm = {0, 0, 0, 0};
#pragma unroll
      for (int kb = 0; kb < 14; kb++) sm = mfma(ones, t[kb], sm);
      const float m = sm[0] * (1.f / D);
      f32x2 v[56], a0 = {0, 0}, a1 = {0, 0};
#pragma unroll
      for (int i = 0; i < 56; i += 2) {
        v[i] = (f32x2){lo(t[i >> 2][i & 3]), hi(t[i >> 2][i & 3])} - m; v[i + 1] = (f32x2){lo(t[i >> 2][(i & 3) + 1]), hi(t[i >> 2][(i & 3) + 1])} - m;
        a0 += v[i] * v[i]; a1 += v[i + 1] * v[i + 1];
      }
      a0 += a1;
      float var = a0.x + a0.y; var = sum16(var, var); var = sum32(var, var);
      const float rs = __builtin_amdgcn_rsqf(var * (1.f / D) + 1e-5f);
#pragma unroll
      for (int i = 0; i < 56; i++) { f32x2 d = v[i] * rs; xn[2 * i] = d.x; xn[2 * i + 1] = d.y; }
    }
#pragma unroll
    for (int i = 0; i < 112; i++) asm volatile("" : "+v"(xn[i]));
    TX(2);
    u32 wa[2][2] = {{sw0, sw0 + 57344}, {sw1, sw1 + 57344}}, ma = L_MP + q * 32, ca = L_MP + 1792 + (l15 >> 3) * 128 + q * 32 + (l15 & 7) * 4;
    asm volatile("" : "+v"(wa[0][0]), "+v"(wa[0][1]), "+v"(wa[1][0]), "+v"(wa[1][1]), "+v"(ma), "+v"(ca));
    lds_wait4(L, L_CNT + 16);
    TX(4);
    f32x4 acc[7];
    {
      v4u wf[2][7], mp[2][2], hf[2];
      float cs[7];
#pragma unroll
      for (int k = 0; k < 2; k++) {
        mp[k][0] = *(LAS v4u*)(L + ma + k * 128); mp[k][1] = *(LAS v4u*)(L + ma + k * 128 + 16);
        if (k == 0) { cs[0] = *(LAS float*)(L + ca); cs[1] = *(LAS float*)(L + ca + 256); }
        SCHED();
#pragma unroll
        for (int et = 0; et < 7; et++) wf[k][et] = *(LAS v4u*)(L + wa[k][et >> 2] + (et & 3) * 14336);
        SCHED();
      }
#pragma unroll
      for (int g = 2; g < 7; g++) cs[g] = *(LAS float*)(L + ca + g * 256);
      SCHED();
      u32 h[8];
#define FM(kb, i, m) { h[i] = m[(i) >> 2][(i) & 3]; fmacb<((kb) & 1) * 8 + (i)>(h[i], cs[(kb) >> 1], xn[(kb) * 8 + (i)]); }
      FM(0, 0, mp[0]) FM(0, 1, mp[0]) FM(0, 2, mp[0]) FM(0, 3, mp[0]) FM(0, 4, mp[0]) FM(0, 5, mp[0]) FM(0, 6, mp[0]) FM(0, 7, mp[0])
      hf[0] = (v4u){pk(uf(h[0]), uf(h[1])), pk(uf(h[2]), uf(h[3])), pk(uf(h[4]), uf(h[5])), pk(uf(h[6]), uf(h[7]))};
      asm volatile("s_nop 1" : "+v"(hf[0]));
      SCHED();
      auto step = [&](auto KB) {
        constexpr int kb = KB.value, c = kb & 1, nx = c ^ 1;
#pragma unroll
        for (int et = 0; et < 7; et++) {
          if (kb == 0) mma0(acc[et], hf[c], wf[c][et]); else mma(acc[et], hf[c], wf[c][et]);
          SCHED();
          if (kb < 12) {
            wf[c][et] = *(LAS v4u*)(L + wa[c][et >> 2] + (et & 3) * 14336 + ((kb + 2) >> 1) * 128);
            if (et < 2) mp[c][et] = *(LAS v4u*)(L + ma + (kb + 2) * 128 + et * 16);
          }
          SCHED();
          if constexpr (kb < 13) {
            if (et == 0) { FM(kb + 1, 0, mp[nx]) FM(kb + 1, 1, mp[nx]) }
            if (et == 1) { FM(kb + 1, 2, mp[nx]) FM(kb + 1, 3, mp[nx]) }
            if (et == 2) { FM(kb + 1, 4, mp[nx]) FM(kb + 1, 5, mp[nx]) }
            if (et == 3) { FM(kb + 1, 6, mp[nx]) FM(kb + 1, 7, mp[nx]) }
            if (et == 4) { hf[nx].x = pk(uf(h[0]), uf(h[1])); hf[nx].y = pk(uf(h[2]), uf(h[3])); }
            if (et == 5) { hf[nx].z = pk(uf(h[4]), uf(h[5])); hf[nx].w = pk(uf(h[6]), uf(h[7])); }
          }
          SCHED();
        }
      };
      sfor<14>(step);
      asm volatile("s_nop 7" : "+v"(acc[0]), "+v"(acc[1]), "+v"(acc[2]), "+v"(acc[3]), "+v"(acc[4]), "+v"(acc[5]), "+v"(acc[6]));
    }
    TX(5);
    {
      const u32 sg = hp ? 0x80008000u : 0u;
      const u32 vo = ((q & 1) * 32 + (tw & 1) * 2 + (q >> 1)) * 1024 + (((sp & 1) * 2 + (tw >> 1)) * 16 + l15) * 16;
      const u32 so = WS_AB + ((((xcd >> 2) * 2 + hp) * 4 + j) * 7 * 32 + ((sp & 15) >> 1) * 4) * 1024;
#pragma unroll
      for (int et = 0; et < 7; et += 2) {
        const int e1 = et < 6 ? et + 1 : et;
        auto a = __builtin_amdgcn_permlane16_swap(pk(acc[et][0], acc[et][1]) ^ sg, pk(acc[e1][0], acc[e1][1]) ^ sg, false, false);
        auto c = __builtin_amdgcn_permlane16_swap(pk(acc[et][2], acc[et][3]) ^ sg, pk(acc[e1][2], acc[e1][3]) ^ sg, false, false);
        st16((v4u){a[0], c[0], a[1], c[1]}, rws, (et < 6 || !(q & 1)) ? vo : OOB, so + et * 32768);
      }
    }
  } else {
    int p0 = tw * 14, np = 14;
    if (crit) { p0 = tw ? tw * 17 - 12 : 0; np = tw ? 17 : 5; }
    if (crit && tw == 0) {
      v4u mw[14]; u32 mc[7];
      __builtin_amdgcn_s_setprio(3);
      mod_load(mw, mc);
      TX(18);
      barrier();
      WAIT7(14, mc);
      GETEP();
      mod_silu(mc);
      WAIT7(7, mw); WAIT7(0, mw + 7);
      mod_finish(mw, mc);
      TX(20);
      __builtin_amdgcn_s_setprio(1);
    } else barrier();
    if (lane < 56) {
      int p = p0 + (((r >> 2) * np) >> 3);
#pragma unroll 1
      for (int i = 0; i < np; i++) {
        dma_rows2(L_WD + p * 1792, (lane ^ (p & 7)) * 16, rd, (j * 112 + p * 2) * 896);
        if (++p == p0 + np) p = p0;
      }
    }
    __builtin_amdgcn_s_setprio(0);
    TX(1);
  }

  const int rot = ROT ? (r >> 2) & 3 : 0;
  v4u A[2][16];
  {
    const rsrc_t rw = mk((kq & 1) ? wim : wre, S * S * 2);
#define PITCH 4096
    const u32 vo = ((lane >> 2) & 3) * 4 * PITCH + (q * 4 + (lane & 3)) * 16;
    const u32 so = (tq * 128 + tw * 32) * PITCH + (kq >> 1) * (PITCH == 4096 ? 2048 : 2048 * 2048) + kw * 1024;
    const u32 f0 = (kw ^ (j & 1)) * 16 * PITCH;
    v4u G[8][4];
    const u32 odd1 = -(lane & 1), odd2 = -((lane >> 1) & 1);
    auto wload = [&](int s) {
      constexpr int OL[8] = {ORD_L};
      const int g = OL[s];
#pragma unroll
      for (int i = 0; i < 4; i++) G[g][i] = ld16(rw, vo, so + ((g + rot) & 3) * 256 + (f0 ^ ((g >> 2) * 16 * PITCH)) + i * PITCH, 0);
      SCHED();
    };
    auto wtr = [&](int s) { constexpr int OL[8] = {ORD_L}; const int g = OL[s]; tr4(G[g], &A[g >> 2][(g & 3) * 4], odd1, odd2); SCHED(); };
    v4u gg[4];
    auto gather = [&]() {
      TX(3);
      if (EARLY == 0) WAIT4(0, gg); else if (EARLY == 1) WAIT4(4, gg); else WAIT4(8, gg);
      GETEP();
      if (!clean<56>(bad4(gg, ep))) {
        _Pragma("unroll 1") for (int it = 0; it < SPIN; it++) { g_load(gg); WAIT4(0, gg); if (clean<56>(bad4(gg, ep))) break; }
      }
      g_put(gg);
      TX(4);
    };
    auto drain = [&]() {
      asm volatile("s_waitcnt vmcnt(%0)" :: "n"(NPRE * 4) : "memory");
      lds_set(L, L_CNT + 32 + tw * 4, 1);
      if (tw == 0) {
        lds_wait4(L, L_CNT + 32);
        __builtin_amdgcn_raw_buffer_store_b64((v2u){(u32)ep, (u32)(ep >> 32)}, rws, lane == 0 ? 0 : OOB,
                                              WS_BF + ((((xcd >> 2) * 2 + hp) * 4 + j) * 16 + (sp & 15)) * 128, SC1);
      }
      TX(6);
    };
    if (!comp) { g_load(gg); if (EARLY == 0) gather(); }
    sfor<8>([&](auto Gi) {
      constexpr int g = Gi.value;
      if (g == NPRE && comp) drain();
      wload(g);
      if (g + 1 == EARLY && !comp) gather();
      if (ILV) { if (g >= LA) wtr(g - LA); }
      else if (g >= LA && g < 7 && !comp) asm volatile("s_waitcnt vmcnt(%0)" :: "n"(LA * 4) : "memory");
    });
    if (NPRE == 8 && comp) drain();
    TX(19);
#pragma unroll
    for (int g = ILV ? 8 - LA : 0; g < 8; g++) wtr(g);
  }
  __builtin_amdgcn_s_setprio(0);
  TS(7); TC(31);
  const u32 lane16 = lane * 16;
  const u32 bsrc = WS_AB + (kq * 4 + j) * 7 * 32768 + tw * 8192;
  if (!comp) {
    lds_wait4(L, L_CNT + 32);
    const u32 fo = lane < 4 ? WS_BF + ((kq * 4 + j) * 16 + tw * 4 + lane) * 128 : OOB;
    _Pragma("unroll 1") for (int it = 0; it < SPIN; it++) {
      FENCE();
      v2u g = __builtin_amdgcn_raw_buffer_load_b64(rws, fo, 0, SC1);
      if (clean<4>(bad(g, ep))) break;
    }
    TX(2);
#pragma unroll
    for (int n = 0; n < 3; n++) {
      dma4k(n * 32768 + (tw >> 1) * 16384 + ((tw * 2 - rot) & 3) * 4096, lane16, rws, bsrc + n * 32768);
      dma4k(n * 32768 + (tw >> 1) * 16384 + ((tw * 2 + 1 - rot) & 3) * 4096, lane16, rws, bsrc + n * 32768 + 4096);
    }
#pragma unroll
    for (int n = 3; n < 7; n++) dma1k<1>(L_PF + wave * 1024, lane16, rws, bsrc + n * 32768 + (r >> 2) * 1024);
    WAITVM(20);
  }
  WAITLGKM0();
  barrier();
  TS(8); TC(16);

  const int o_self = kq * 4 + j;
  const u32 cm_vo = (tw * 4 + (kw ^ (j & 1)) * 2 + (l15 >> 3)) * 28672 + q * 64 + (lane & 7) * 8;
  const u32 cm_so = WS_CM + tq * 16 * 28672 + o_self * 7 * 256;
  v4u xr, wrr, gm;
  {
    u32 ba = kw * 16384 + lane16;
    const u32 dsrc = WS_AB + o_self * 7 * 32768 + kw * 16384 + ((tw + rot) & 3) * 4096;
    v4u B[4];
#pragma unroll
    for (int k = 0; k < 4; k++) B[k] = *(LAS v4u*)(L + ba + k * 1024);
    f32x4 a0[2], a1[2], pr;
    float mv, ma[8]; v4u lw; u32 lc;
#pragma unroll
    for (int n = 0; n < 7; n++) {
      const int c = n & 1;
      const u32 bn = (ba + 32768) & 0x1ffff;
#pragma unroll
      for (int k = 0; k < 16; k++) {
        if (k == 12) {
          if (n == 0) asm volatile("s_waitcnt vmcnt(%0)" :: "n"(10 + BPF * 4) : "memory"); else if (n == 1) asm volatile("s_waitcnt vmcnt(%0)" :: "n"(7 + BPF * 4) : "memory");
          else if (n == 2) WAITVM(6); else if (n == 3) WAITVM(5); else if (n == 4) WAITVM(8); else if (n == 5) WAITVM(5);
          barrier();
          if (n > 0) pr = *(LAS f32x4*)(L + L_RED + (c ^ 1) * 8192 + (wave ^ 4) * 1024 + lane16);
        }
        if (k == 0) { mma0(a0[c], B[0], A[0][0]); mma0(a1[c], B[0], A[1][0]); }
        else { mma(a0[c], B[k & 3], A[0][k]); mma(a1[c], B[k & 3], A[1][k]); }
        SCHED();
        if (n < 6 || k < 12) B[k & 3] = k < 12 ? *(LAS v4u*)(L + ba + (k + 4) * 1024) : *(LAS v4u*)(L + bn + (k - 12) * 1024);
        if (k == 2 && n > 0) { asm volatile("" : "+v"(a1[c ^ 1])); *(LAS f32x4*)(L + L_RED + (c ^ 1) * 8192 + wave * 1024 + lane16) = a1[c ^ 1]; }
        if (k == 14 && n > 0) { pr += a0[c ^ 1]; __builtin_amdgcn_raw_buffer_store_b64((v2u){pk(pr[0], pr[1]), pk(pr[2], pr[3])}, rws, cm_vo, cm_so + (n - 1) * 256, SC1); }
        if ((k == 13 || k == 15) && n < 4) dma1k<1>(((n + 3) & 3) * 32768 + wave * 4096 + (k - 13) * 512, lane16, rws, dsrc + (n + 3) * 32768 + (k - 13) * 512);
        if ((k == 1 || k == 3) && n >= 1 && n < 5) dma1k<1>(((n + 2) & 3) * 32768 + wave * 4096 + (k + 3) * 512, lane16, rws, dsrc + (n + 2) * 32768 + (k + 3) * 512);
        if (n == 4 && (k == 5 || k == 7)) {
          const int id = (r * 8 + wave) * 2 + (k == 7);
          dma1k<0>(L_PF + wave * 1024, PFC id < 448 ? (lane >> 5) * 3584 + (lane & 31) * 16 : OOB, mk(id >= 224 ? wu : wg, D * F * 2),
                   (id >= 224 ? id - 224 : id) * 7168 + ((xcd * 448) & ~127));
        }
        if (n == 4 && k == 9) dma1k<0>(L_PF + wave * 1024, PFC r * 8 + wave < 196 ? lane16 : OOB, mk(wdn, D * F * 2), xcd * 224 * 896 + (r * 8 + wave) * 1024);
        if (n == 5 && k == 13) {
          xr = ld16(mk(x, S * D * 2), lane < 56 ? lane16 : OOB, (tq * 128 + o_self * 8 + wave) * 896, 0);
          wrr = ld16(mk(wr, D * 2), lane16, 0, 0);
        }
        if (n == 0 && k == 1) {
          dma1k<0>(L_PF + wave * 1024, NOP_ r < 28 ? (lane >> 5) * 5376 + (lane & 31) * 16 : OOB, mk(wmod, D * 6 * D * 2), (r * 16 + wave * 2) * 5376 + ((1792 + xcd * 448) & ~127));
          ld2a(lc, mk(cv, D * 2), lane * 2, wave * 128);
        }
        if (n == 1 && k == 8) ld16a<0>(lw, mk(wmod, D * 6 * D * 2), NOW_ r < 28 && wave < 7 ? lane * 5376 : OOB, wave * 64 * 5376 + 1792 + xcd * 448 + r * 16);
        if (n == 3 && k == 2) { asm volatile("" : "+v"(lc)); mv = lo(lc); ma[0] = 1.f + __expf(-mv); }
        if (n == 3 && k == 4) mv = rbf(mv * __builtin_amdgcn_rcpf(ma[0]));
        if (n == 3 && k == 13) asm volatile("" : "+v"(lw));
        if (n == 3 && k >= 14) { const int h = (k - 14) * 2; ma[2 * h] = mv * lo(lw[h]); ma[2 * h + 1] = mv * hi(lw[h]); ma[2 * h + 2] = mv * lo(lw[h + 1]); ma[2 * h + 3] = mv * hi(lw[h + 1]); }
        if (n == 4 && k < 2) { ma[2 * k] = sum32(ma[2 * k], ma[2 * k + 4]); ma[2 * k + 1] = sum32(ma[2 * k + 1], ma[2 * k + 5]); }
        if (n == 4 && k == 2) { ma[0] = sum16(ma[0], ma[2]); ma[1] = sum16(ma[1], ma[3]); }
        if (n == 4 && k == 3) { ma[0] += dpp<ROR8>(ma[0]); ma[1] += dpp<ROR8>(ma[1]); ma[0] += dpp<ROR4>(ma[0]); ma[1] += dpp<ROR4>(ma[1]); }
        if (n == 4 && k == 4) { ma[0] += dpp<QP2>(ma[0]); ma[1] += dpp<QP2>(ma[1]); ma[0] += dpp<QP1>(ma[0]); ma[1] += dpp<QP1>(ma[1]); }
        if (n == 4 && k == 6) *(LAS f32x2*)(L + L_MPART + wave * 32 + q * 8) = (f32x2){ma[0], ma[1]};
        if (n == 4 && k == 13) mv = *(LAS float*)(L + L_MPART + lane * 4);
        if (n == 5 && k == 2) { mv += dpp<ROR8>(mv); mv = sum16(mv, mv); mv = sum32(mv, mv); }
        if (n == 5 && k == 4) {
          const u32 u = pk(mv, dpp<QP1>(mv));
          st16((v4u){u, fu(dpp<QP2>(uf(u))), (u32)ep, (u32)(ep >> 32)}, rws, wave == 7 && r < 28 && (lane & 59) == 0 ? lane * 4 : OOB, WS_MODL + (xcd * 28 + r) * 32);
        }
        if (n == 6 && k == 13) gm = ld16(rws, wave < 7 ? lane16 : OOB, WS_MODL + wave * 1024, 1);
        SCHED();
      }
      ba = bn;
    }
    TS(9); TC(17);
    asm volatile("s_nop 7" : "+v"(a0[0]), "+v"(a1[0]));
    *(LAS f32x4*)(L + L_RED + wave * 1024 + lane16) = a1[0];
    WAITLGKM0(); barrier();
    pr = *(LAS f32x4*)(L + L_RED + (wave ^ 4) * 1024 + lane16);
    pr += a0[0];
    __builtin_amdgcn_raw_buffer_store_b64((v2u){pk(pr[0], pr[1]), pk(pr[2], pr[3])}, rws, cm_vo, cm_so + 6 * 256, SC1);
  }
  WAITVM(0); barrier();
  if (wave == 7) __builtin_amdgcn_raw_buffer_store_b64((v2u){(u32)ep, (u32)(ep >> 32)}, rws, lane == 0 ? 0 : OOB, WS_CF + (tq * 16 + o_self) * 128, SC1);
  else {
    _Pragma("unroll 1") for (int it = 0; it < SPIN; it++) {
      if (clean<64>(bad(gm, ep))) break;
      FENCE();
      gm = ld16(rws, lane16, WS_MODL + wave * 1024, 1);
    }
    *(LAS f32x4*)(L + L_MR + wave * 1024 + lane16) = (f32x4){lo(gm.x), hi(gm.x), lo(gm.y), hi(gm.y)};
  }
  TS(10);
  WAITLGKM0(); barrier();
  const bool act = lane < 56;
  f32x2 ga[4], sb[4], c1[4], gb[4], xv[4], cw[4];
  float scw, ssw;
  {
    const u32 mo = L_MR + (act ? lane * 32 : 0);
    float p0 = 0, p1 = 0;
#pragma unroll
    for (int k = 0; k < 4; k++) {
      ga[k] = *(LAS f32x2*)(L + mo + k * 8); sb[k] = *(LAS f32x2*)(L + mo + 1792 + k * 8);
      const f32x2 cb = *(LAS f32x2*)(L + mo + 3584 + k * 8); gb[k] = *(LAS f32x2*)(L + mo + 5376 + k * 8);
      const u32 t = pk(1.f + cb.x, 1.f + cb.y);
      c1[k] = (f32x2){lo(t), hi(t)};
      xv[k] = (f32x2){lo(xr[k]), hi(xr[k])};
      const f32x2 w = {lo(wrr[k]), hi(wrr[k])};
      cw[k] = c1[k] * w;
      if (!act) ga[k] = (f32x2){0, 0};
      p0 += cw[k].x + cw[k].y; p1 += sb[k].x * w.x + sb[k].y * w.y;
    }
    scw = wsum(p0); ssw = wsum(p1);
#pragma unroll
    for (int k = 0; k < 4; k++) asm volatile("" : "+v"(ga[k]), "+v"(sb[k]), "+v"(c1[k]), "+v"(gb[k]), "+v"(xv[k]), "+v"(cw[k]));
    asm volatile("" : "+v"(scw), "+v"(ssw));
  }
  if (wave == 7) {
    const u32 fo = lane < 16 ? WS_CF + (tq * 16 + lane) * 128 : OOB;
    _Pragma("unroll 1") for (int it = 0; it < SPIN; it++) {
      FENCE();
      v2u g = __builtin_amdgcn_raw_buffer_load_b64(rws, fo, 0, SC1);
      if (clean<16>(bad(g, ep))) break;
    }
  }
  barrier();
  TS(11);
  if (wave < 7) {
    v4u p[4];
#pragma unroll
    for (int k = 0; k < 4; k++) p[k] = ld16(rws, lane16, WS_CM + (tq * 16 + o_self) * 28672 + k * 7168 + wave * 1024, 1);
    f32x4 y0, y1;
    y0[0] = (lo(p[0].x) + lo(p[1].x)) + (lo(p[2].x) + lo(p[3].x)); y0[1] = (hi(p[0].x) + hi(p[1].x)) + (hi(p[2].x) + hi(p[3].x));
    y0[2] = (lo(p[0].y) + lo(p[1].y)) + (lo(p[2].y) + lo(p[3].y)); y0[3] = (hi(p[0].y) + hi(p[1].y)) + (hi(p[2].y) + hi(p[3].y));
    y1[0] = (lo(p[0].z) + lo(p[1].z)) + (lo(p[2].z) + lo(p[3].z)); y1[1] = (hi(p[0].z) + hi(p[1].z)) + (hi(p[2].z) + hi(p[3].z));
    y1[2] = (lo(p[0].w) + lo(p[1].w)) + (lo(p[2].w) + lo(p[3].w)); y1[3] = (hi(p[0].w) + hi(p[1].w)) + (hi(p[2].w) + hi(p[3].w));
    const u32 yo = L_Y + (lane & 3) * 2 * 1808 + (wave * 16 + (lane >> 2)) * 16;
    *(LAS f32x4*)(L + yo) = y0; *(LAS f32x4*)(L + yo + 1808) = y1;
  }
  WAITLGKM0(); barrier();
  TS(12);
  const rsrc_t rout = mk(out, S * D * 2);
  const int myrow = tq * 128 + o_self * 8 + wave;
  const int mi = xcd * 7 + r;
  v4u mg[8], md[4];
  {
    const f32x4 ya = *(LAS f32x4*)(L + L_Y + wave * 1808 + (act ? lane * 32 : 0)), yb = *(LAS f32x4*)(L + L_Y + wave * 1808 + (act ? lane * 32 : 0) + 16);
    f32x2 x1[4], d[4];
    x1[0] = ga[0] * (f32x2){ya[0], ya[1]} + xv[0]; x1[1] = ga[1] * (f32x2){ya[2], ya[3]} + xv[1];
    x1[2] = ga[2] * (f32x2){yb[0], yb[1]} + xv[2]; x1[3] = ga[3] * (f32x2){yb[2], yb[3]} + xv[3];
    const float piv = uf(__builtin_amdgcn_readfirstlane(fu(x1[0].x)));
    const float pv = act ? piv : 0.f;
#pragma unroll
    for (int k = 0; k < 4; k++) d[k] = x1[k] - pv;
    const f32x2 t1 = (d[0] + d[1]) + (d[2] + d[3]);
    const f32x2 t2 = d[0] * d[0] + d[1] * d[1] + d[2] * d[2] + d[3] * d[3];
    const f32x2 t3 = d[0] * cw[0] + d[1] * cw[1] + d[2] * cw[2] + d[3] * cw[3];
    float s1 = t1.x + t1.y, s2 = t2.x + t2.y, s3 = t3.x + t3.y;
    s1 += dpp<QP1>(s1); s2 += dpp<QP1>(s2); s3 += dpp<QP1>(s3);
    s1 += dpp<QP2>(s1); s2 += dpp<QP2>(s2); s3 += dpp<QP2>(s3);
    s1 += dpp<ROR4>(s1); s2 += dpp<ROR4>(s2); s3 += dpp<ROR4>(s3);
    s1 += dpp<ROR8>(s1); s2 += dpp<ROR8>(s2); s3 += dpp<ROR8>(s3);
    const float w = sum32(sum16(s1, s2), sum16(s3, s3));
    const float m = uf(__builtin_amdgcn_readlane(fu(w), 0)) * (1.f / D);
    const float var = fmaxf(uf(__builtin_amdgcn_readlane(fu(w), 16)) * (1.f / D) - m * m, 0.f);
    const float rs = __builtin_amdgcn_rsqf(var + 1e-5f);
    const float rsc = rs * (uf(__builtin_amdgcn_readlane(fu(w), 32)) - m * scw) + ssw;
    if (lane == 0) *(LAS float*)(L + L_RB + wave * 4) = rsc;
    WAITLGKM0(); barrier();
    const float rv = *(LAS float*)(L + L_RB + (lane & 7) * 4);
    float mx = fmaxf(rv, dpp<QP1>(rv)); mx = fmaxf(mx, dpp<QP2>(mx)); mx = fmaxf(mx, dpp<ROR4>(mx));
    const u64 bal = __builtin_amdgcn_ballot_w64(rv == mx);
    const bool best = (bal ? __builtin_ctzll(bal) : 0) == wave;
    if (best) st16((v4u){fu(rsc), (u32)myrow, (u32)ep, (u32)(ep >> 32)}, rws, lane == 0 ? 0 : OOB, WS_DG + b * 64);
    u32 h2p[4]; f32x2 base[4];
#pragma unroll
    for (int k = 0; k < 4; k++) {
      const f32x2 h = ((d[k] - m) * rs) * c1[k] + sb[k];
      h2p[k] = pk(h.x, h.y);
      base[k] = gb[k] * (f32x2){lo(h2p[k]), hi(h2p[k])} + x1[k];
    }
    const v4u ov = {pk(base[0].x, base[0].y), pk(base[1].x, base[1].y), pk(base[2].x, base[2].y), pk(base[3].x, base[3].y)};
    if (best) {
      st16((v4u){h2p[0], h2p[1], (u32)ep, (u32)(ep >> 32)}, rws, act ? lane * 32 : OOB, WS_DH + b * 2048);
      st16((v4u){h2p[2], h2p[3], (u32)ep, (u32)(ep >> 32)}, rws, act ? lane * 32 + 16 : OOB, WS_DH + b * 2048);
      st16((v4u){fu(base[0].x), fu(base[0].y), fu(base[1].x), fu(base[1].y)}, rws, act ? lane * 32 : OOB, WS_DB + b * 2048);
      st16((v4u){fu(base[2].x), fu(base[2].y), fu(base[3].x), fu(base[3].y)}, rws, act ? lane * 32 + 16 : OOB, WS_DB + b * 2048);
    }
    st16(ov, rout, act ? lane16 : OOB, myrow * 896);
    if (best) {
      WAITVM(0);
      __builtin_amdgcn_raw_buffer_store_b64((v2u){(u32)ep, (u32)(ep >> 32)}, rws, lane == 0 ? 0 : OOB, WS_DF + b * 128, SC1);
    }
  }
  TS(13);
  if (b < NMLP) {
    {
      const rsrc_t rg = mk(wg, D * F * 2), ru = mk(wu, D * F * 2), rdn = mk(wdn, D * F * 2);
      const u32 vo = wave < 7 ? (lane >> 2) * 4 * 3584 + (lane & 3) * 16 : OOB;
#pragma unroll
      for (int i = 0; i < 4; i++) {
        mg[i] = ld16(rg, vo, (wave * 64 + i) * 3584 + mi * 64, 0);
        mg[4 + i] = ld16(ru, vo, (wave * 64 + i) * 3584 + mi * 64, 0);
      }
#pragma unroll
      for (int i = 0; i < 4; i++) md[i] = ld16(rdn, lane < 56 ? lane16 : OOB, (mi * 32 + wave * 4 + i) * 896, 0);
    }
    float gw[64], dw[32];
#pragma unroll
    for (int i = 0; i < 8; i++)
#pragma unroll
      for (int k = 0; k < 4; k++) { gw[i * 8 + 2 * k] = lo(mg[i][k]); gw[i * 8 + 2 * k + 1] = hi(mg[i][k]); }
#pragma unroll
    for (int i = 0; i < 4; i++)
#pragma unroll
      for (int k = 0; k < 4; k++) { dw[i * 8 + 2 * k] = lo(md[i][k]); dw[i * 8 + 2 * k + 1] = hi(md[i][k]); }
#pragma unroll
    for (int i = 0; i < 64; i++) asm volatile("" : "+v"(gw[i]));
#pragma unroll
    for (int i = 0; i < 32; i++) asm volatile("" : "+v"(dw[i]));
    {
      v4u g;
      _Pragma("unroll 1") for (int it = 0; it < SPIN; it++) { FENCE(); g = ld16(rws, lane < 32 ? lane * 64 : OOB, WS_DG + wave * 2048, 1); if (clean<32>(bad(g, ep))) break; }
      const float rv = lane < 32 ? uf(g.x) : -__builtin_inff();
      const float mx = wmax(rv);
      const u64 bal = __builtin_amdgcn_ballot_w64(rv == mx && lane < 32);
      const int first = bal ? __builtin_ctzll(bal) : 0;
      if (lane == first) *(LAS v4u*)(L + L_RB2 + wave * 16) = (v4u){g.x, g.y, (u32)(wave * 32 + lane), 0};
    }
    WAITLGKM0(); barrier();
    int win, wrow; float rwin;
    {
      const v4u e = *(LAS v4u*)(L + L_RB2 + (lane & 7) * 16);
      float mx = uf(e.x); mx = fmaxf(mx, dpp<QP1>(mx)); mx = fmaxf(mx, dpp<QP2>(mx)); mx = fmaxf(mx, dpp<ROR4>(mx));
      const u64 bal = __builtin_amdgcn_ballot_w64(uf(e.x) == mx);
      const int first = bal ? __builtin_ctzll(bal) : 0;
      win = __builtin_amdgcn_readlane(e.z, first); wrow = __builtin_amdgcn_readlane(e.y, first); rwin = uf(__builtin_amdgcn_readlane(e.x, first));
    }
    TS(14);
    v4u bs = {0, 0, 0, 0};
    if (wave < 7) {
      v4u g;
      _Pragma("unroll 1") for (int it = 0; it < SPIN; it++) { FENCE(); g = ld16(rws, (lane >> 2) * 16, WS_DH + win * 2048 + wave * 256, 1); if (clean<64>(bad(g, ep))) break; }
      const float hv[4] = {lo(g.x), hi(g.x), lo(g.y), hi(g.y)};
      float a[16];
#pragma unroll
      for (int k = 0; k < 8; k++) {
        a[k] = fmaf(hv[3], gw[24 + k], fmaf(hv[2], gw[16 + k], fmaf(hv[1], gw[8 + k], hv[0] * gw[k])));
        a[8 + k] = fmaf(hv[3], gw[56 + k], fmaf(hv[2], gw[48 + k], fmaf(hv[1], gw[40 + k], hv[0] * gw[32 + k])));
      }
#pragma unroll
      for (int k = 0; k < 8; k++) a[k] = sum32(a[k], a[k + 8]);
#pragma unroll
      for (int k = 0; k < 4; k++) a[k] = sum16(a[k], a[k + 4]);
#pragma unroll
      for (int k = 0; k < 4; k++) { a[k] += dpp<ROR8>(a[k]); a[k] += dpp<ROR4>(a[k]); }
      if ((lane & 12) == 0) *(LAS f32x4*)(L + L_GU + wave * 256 + (lane >> 5) * 128 + (lane & 3) * 32 + ((lane >> 4) & 1) * 16) = (f32x4){a[0], a[1], a[2], a[3]};
    } else {
      _Pragma("unroll 1") for (int it = 0; it < SPIN; it++) {
        FENCE();
        v2u g = __builtin_amdgcn_raw_buffer_load_b64(rws, 0, WS_DF + win * 128, SC1);
        if (clean<64>(bad(g, ep))) break;
      }
      FENCE();
      v2u t = __builtin_amdgcn_raw_buffer_load_b64(rws, (lane & 3) * 8, WS_DB + win * 2048 + mi * 32, SC1);
      const v2u gbv = *(LAS v2u*)(L + L_MR + 5376 + mi * 32 + (lane & 3) * 8);
      bs = (v4u){t.x, t.y, gbv.x, gbv.y};
    }
    WAITLGKM0(); barrier();
    {
      float v = (lane >> 3) < 7 ? *(LAS float*)(L + L_GU + (lane >> 3) * 256 + ((lane >> 2) & 1) * 128 + wave * 16 + (lane & 3) * 4) : 0.f;
      v += dpp<ROR8>(v); v = sum16(v, v); v = sum32(v, v);
      v = rbf(v);
      const float u = dpp<ROR4>(v);
      const float av = rbf(rbf(v * sigm(v)) * u);
      float y[8];
#pragma unroll
      for (int i = 0; i < 4; i++) {
        const float s = uf(__builtin_amdgcn_readlane(fu(av), i));
#pragma unroll
        for (int k = 0; k < 8; k++) y[k] = i ? fmaf(s, dw[i * 8 + k], y[k]) : s * dw[k];
      }
      if (act) { *(LAS f32x4*)(L + L_DN + wave * 1792 + lane * 32) = (f32x4){y[0], y[1], y[2], y[3]}; *(LAS f32x4*)(L + L_DN + wave * 1792 + lane * 32 + 16) = (f32x4){y[4], y[5], y[6], y[7]}; }
    }
    WAITLGKM0(); barrier();
    if (threadIdx.x < 224) {
      f32x2 t = *(LAS f32x2*)(L + L_DN + threadIdx.x * 8);
#pragma unroll
      for (int k = 1; k < 8; k++) t += *(LAS f32x2*)(L + L_DN + k * 1792 + threadIdx.x * 8);
      st16((v4u){fu(t.x), fu(t.y), (u32)ep, (u32)(ep >> 32)}, rws, threadIdx.x * 16, WS_EG + mi * 3584);
    }
    if (wave == 7) {
      const u32 vo = (lane >> 2) * 3584 + (lane & 3) * 16;
      v4u g[4];
      _Pragma("unroll 1") for (int it = 0; it < SPIN; it++) {
        FENCE();
#pragma unroll
        for (int i = 0; i < 4; i++) g[i] = ld16(rws, (i < 3 || lane < 32) ? vo : OOB, WS_EG + i * 16 * 3584 + mi * 64, 1);
        if (clean<64>(bad(g[0], ep) | bad(g[1], ep) | bad(g[2], ep) | (lane < 32 ? bad(g[3], ep) : 0u))) break;
      }
      float y0 = (uf(g[0].x) + uf(g[1].x)) + (uf(g[2].x) + uf(g[3].x)), y1 = (uf(g[0].y) + uf(g[1].y)) + (uf(g[2].y) + uf(g[3].y));
      y0 += dpp<ROR4>(y0); y1 += dpp<ROR4>(y1); y0 += dpp<ROR8>(y0); y1 += dpp<ROR8>(y1);
      y0 = sum16(y0, y0); y1 = sum16(y1, y1); y0 = sum32(y0, y0); y1 = sum32(y1, y1);
      const float sg = sigm(rwin);
      const u32 o = pk(fmaf(uf(bs.z), y0 * sg, uf(bs.x)), fmaf(uf(bs.w), y1 * sg, uf(bs.y)));
      __builtin_amdgcn_raw_buffer_store_b32(o, rout, lane < 4 ? lane * 4 : OOB, wrow * 896 + mi * 16, SC1);
      if (b == 0) __builtin_amdgcn_raw_buffer_store_b64((v2u){(u32)ep, (u32)(ep >> 32)}, rws, lane == 0 ? 0 : OOB, 0, SC1);
    }
  }
}
}

extern "C" void kernel_launch(void* const* d_in, const int* in_sizes, int n_in, void* d_out, int out_size, void* d_ws, size_t ws_size,
                              hipStream_t stream) {
  (void)hipFuncSetAttribute((const void*)fused_kernel, hipFuncAttributeMaxDynamicSharedMemorySize, L_SIZE);
  fused_kernel<<<256, 512, L_SIZE, stream>>>((const char*)d_in[0], (const char*)d_in[1], (const char*)d_in[2], (const char*)d_in[3],
      (const char*)d_in[4], (const char*)d_in[6], (const char*)d_in[7], (const char*)d_in[8], (const char*)d_in[9], (const char*)d_in[10],
      (const char*)d_in[11], (char*)d_out, (char*)d_ws);
}
```

```cpp
#include <hip/hip_runtime.h>
#include <hip/hip_bf16.h>

typedef unsigned u32;
typedef unsigned long long u64;
typedef u32 v4u __attribute__((ext_vector_type(4)));
typedef u32 v2u __attribute__((ext_vector_type(2)));
typedef u32 v16u __attribute__((ext_vector_type(16)));
typedef __bf16 bfx2 __attribute__((ext_vector_type(2)));
typedef __bf16 bfx8 __attribute__((ext_vector_type(8)));
typedef float f32x4 __attribute__((ext_vector_type(4)));
typedef float f32x2 __attribute__((ext_vector_type(2)));
typedef __amdgpu_buffer_rsrc_t rsrc_t;
#define LAS __attribute__((address_space(3)))
#define DEV __device__ __forceinline__
#define FENCE() asm volatile("" ::: "memory")
#define WAITVM(n) asm volatile("s_waitcnt vmcnt(" #n ")" ::: "memory")
#define WAITLGKM0() asm volatile("s_waitcnt lgkmcnt(0)" ::: "memory")
#define SCHED() __builtin_amdgcn_sched_barrier(0)

namespace {
constexpr int S = 2048, D = 448, F = 1792;
constexpr int SC1 = 16;
constexpr u32 OOB = 0x7ff00000u;
constexpr int SPIN = 1 << 17;
constexpr u32 WS_BF = 16384;
constexpr u32 WS_CF = 23u << 18;
constexpr u32 WS_DF = 81920;
constexpr u32 WS_DG = 47u << 17;
constexpr u32 WS_MODG = 11u << 19;
constexpr u32 WS_MODL = 180224;
constexpr u32 WS_DH = 196608;
constexpr u32 WS_DB = WS_DH + 256 * 2048;
constexpr u32 WS_EG = WS_DB + 256 * 4096;
constexpr u32 WS_AB = 2u << 20;
constexpr u32 WS_CM = 6u << 20;
constexpr u32 WS_DBG = 14u << 20;
constexpr u32 WS_SIZE = 16u << 20;
constexpr u32 L_WD = 0, L_X = 100352, L_MP = 157696, L_CNT = 161280, L_RB = 161344, L_RB2 = 161408, L_MPART = 161536;
#define EARLY 1
#define NPRE 2
#define CRIT 2
#define WFLAG 2
#define TA0 47
#define TA1 51
#define TB 64
#define S2L 0
#define MRB 0
#define GSP 0
#define USL 0
#define CPD 3
#define DSL 10
#define EPD 3
#define ESL 3
#define NDP 0
#define MREP 1
#define RSTR 20480
#define GEP 1
#define GSY 2
#define NP0 8
#define UNT 0
#define LAV 1, 2, 2, 2, 1, 1, 1
#define PFS 2, 3, 5, 4
#define DPD 2
#define EMK 0
#define EIN 20
#define DLP 0
#define CRP 32768
#define DRP 16384
#define BPD 1
#define BSL 5
#define DMK 0
#define DGS 64
#define TCB 1
#define ROT 1
#define PRX 2
#define PREW 0
#define ILV 0
#define ORD_L 0, 4, 1, 5, 2, 6, 3, 7
constexpr u32 L_RED = 131072, L_Y = 131072, L_GU = 131072, L_MR = 32768, L_G = 0, L_U = 98304, L_D = 65536;
constexpr u32 L_SIZE = 163840, L_JUNK = 162816;
#define FINA 0
#define CPF 1
constexpr int NMLP = 56;

DEV rsrc_t mk(const void* p, u32 bytes) { return __builtin_amdgcn_make_buffer_rsrc((void*)p, 0, bytes, 0x00020000); }
DEV v4u ld16(rsrc_t r, u32 v, u32 s, int aux) { return aux ? __builtin_amdgcn_raw_buffer_load_b128(r, v, s, SC1) : __builtin_amdgcn_raw_buffer_load_b128(r, v, s, 0); }
DEV void st16(v4u d, rsrc_t r, u32 v, u32 s) {
  __builtin_amdgcn_raw_buffer_store_b128(d, r, v, s, SC1);
  asm volatile("s_nop 1" :: "v"(d) : "memory");
}
DEV float lo(u32 w) { return __builtin_bit_cast(float, w << 16); }
DEV float hi(u32 w) { return __builtin_bit_cast(float, w & 0xffff0000u); }
DEV u32 pk(float a, float b) { f32x2 v = {a, b}; return __builtin_bit_cast(u32, __builtin_convertvector(v, bfx2)); }
DEV bfx2 bf2(u32 w) { return __builtin_bit_cast(bfx2, w); }
DEV float rbf(float a) { return __builtin_bit_cast(float, pk(0.f, a)); }
DEV u32 fu(float a) { return __builtin_bit_cast(u32, a); }
DEV float uf(u32 a) { return __builtin_bit_cast(float, a); }
template <int C> DEV float dpp(float v) { return uf(__builtin_amdgcn_update_dpp(0u, fu(v), C, 0xf, 0xf, true)); }
constexpr int QP1 = 0xB1, QP2 = 0x4E, ROR4 = 0x124, ROR8 = 0x128;
DEV float sum16(float a, float b) { auto s = __builtin_amdgcn_permlane16_swap(fu(a), fu(b), false, false); return uf(s[0]) + uf(s[1]); }
DEV float sum32(float a, float b) { auto s = __builtin_amdgcn_permlane32_swap(fu(a), fu(b), false, false); return uf(s[0]) + uf(s[1]); }
DEV float max16(float a) { auto s = __builtin_amdgcn_permlane16_swap(fu(a), fu(a), false, false); return fmaxf(uf(s[0]), uf(s[1])); }
DEV float max32(float a) { auto s = __builtin_amdgcn_permlane32_swap(fu(a), fu(a), false, false); return fmaxf(uf(s[0]), uf(s[1])); }
DEV float wsum(float v) { v += dpp<QP1>(v); v += dpp<QP2>(v); v += dpp<0x141>(v); v += dpp<0x140>(v); v = sum16(v, v); return sum32(v, v); }
DEV float wmax(float v) { v = fmaxf(v, dpp<QP1>(v)); v = fmaxf(v, dpp<QP2>(v)); v = fmaxf(v, dpp<ROR4>(v)); v = fmaxf(v, dpp<ROR8>(v)); return max32(max16(v)); }
DEV float sigm(float v) { return __builtin_amdgcn_rcpf(1.f + __expf(-v)); }
DEV u64 bad(v2u g, u64 ep) { return __builtin_amdgcn_ballot_w64(__builtin_bit_cast(u64, g) != ep); }
DEV u64 bad(v4u g, u64 ep) { return bad((v2u){g.z, g.w}, ep); }
DEV u64 bad4(const v4u* g, u64 ep) { return (bad(g[0], ep) | bad(g[1], ep)) | (bad(g[2], ep) | bad(g[3], ep)); }
template <int N> DEV bool clean(u64 b) { return (b & (N == 64 ? ~0ull : (1ull << (N & 63)) - 1)) == 0; }
DEV f32x4 mfma(v4u a, v4u b, f32x4 c) { return __builtin_amdgcn_mfma_f32_16x16x32_bf16(__builtin_bit_cast(bfx8, a), __builtin_bit_cast(bfx8, b), c, 0, 0, 0); }
DEV void mma(f32x4& c, v4u a, v4u b) { asm volatile("v_mfma_f32_16x16x32_bf16 %0, %1, %2, %0" : "+v"(c) : "v"(a), "v"(b)); }
DEV void mma0(f32x4& c, v4u a, v4u b) { asm volatile("v_mfma_f32_16x16x32_bf16 %0, %1, %2, 0" : "=v"(c) : "v"(a), "v"(b)); }
template <int N> DEV void fmacb(u32& d, float c, float v) { asm volatile("v_fmac_f32_dpp %0, %1, %2 row_newbcast:%3 row_mask:0xf bank_mask:0xf" : "+v"(d) : "v"(c), "v"(v), "n"(N)); }
template <int V> struct IC { static constexpr int value = V; };
template <int N, int I = 0, class Fn> DEV void sfor(Fn&& f) { if constexpr (I < N) { f(IC<I>{}); sfor<N, I + 1>(f); } }
DEV void barrier() { FENCE(); __builtin_amdgcn_s_barrier(); FENCE(); }

template <int SYS> DEV void ld16a(v4u& d, rsrc_t r, u32 v, u32 s) {
  if (SYS) asm volatile("buffer_load_dwordx4 %0, %1, %2, %3 offen sc1" : "=&v"(d) : "v"(v), "s"(r), "s"(s) : "memory");
  else asm volatile("buffer_load_dwordx4 %0, %1, %2, %3 offen" : "=&v"(d) : "v"(v), "s"(r), "s"(s) : "memory");
}
DEV void ld4a(u32& d, rsrc_t r, u32 v, u32 s) { asm volatile("buffer_load_dword %0, %1, %2, %3 offen" : "=&v"(d) : "v"(v), "s"(r), "s"(s) : "memory"); }
DEV void ldepa(v2u& d, rsrc_t r) { asm volatile("buffer_load_dwordx2 %0, off, %1, 0 sc1" : "=&v"(d) : "s"(r) : "memory"); }
DEV void ld2a(u32& d, rsrc_t r, u32 v, u32 s) { asm volatile("buffer_load_ushort %0, %1, %2, %3 offen" : "=&v"(d) : "v"(v), "s"(r), "s"(s) : "memory"); }
DEV float vmul(float a, float b) { float d; asm volatile("v_mul_f32 %0, %1, %2" : "=v"(d) : "v"(a), "v"(b)); return d; }
DEV float vdot(u32 a, u32 b) { float d; asm volatile("v_dot2_f32_bf16 %0, %1, %2, 0" : "=v"(d) : "v"(a), "v"(b)); return d; }
DEV float vmov(float a) { float d; asm volatile("v_mov_b32 %0, %1" : "=v"(d) : "v"(a)); return d; }
DEV float vadd(float a, float b) { float d; asm volatile("v_add_f32 %0, %1, %2" : "=v"(d) : "v"(a), "v"(b)); return d; }
#define VADD_DPP(a, c) asm volatile("v_add_f32_dpp %0, %0, %0 " c " row_mask:0xf bank_mask:0xf" : "+v"(a))
#define VSWAP(n, a, b) asm volatile("v_permlane" #n "_swap_b32 %0, %1" : "+v"(a), "+v"(b))
DEV void colsum8(float& a0, float& a1, v4u w, u32 s0, u32 s1, int skip) {
  float a2, a3, a4, a5, a6, a7;
#define DP(c) "v_add_f32_dpp %0, %0, %0 " c " row_mask:0xf bank_mask:0xf\n\tv_add_f32_dpp %1, %1, %1 " c " row_mask:0xf bank_mask:0xf\n\t"
  asm volatile("s_cmp_lg_u32 %14, 0\n\ts_cbranch_scc1 1f\n\tv_dot2_f32_bf16 %0, %8, %12, 0\n\tv_dot2_f32_bf16 %4, %10, %12, 0\n\tv_dot2_f32_bf16 %1, %8, %13, 0\n\tv_dot2_f32_bf16 %5, %10, %13, 0\n\t"
    "v_dot2_f32_bf16 %2, %9, %12, 0\n\tv_dot2_f32_bf16 %6, %11, %12, 0\n\tv_dot2_f32_bf16 %3, %9, %13, 0\n\tv_dot2_f32_bf16 %7, %11, %13, 0\n\t"
    "v_permlane32_swap_b32 %0, %4\n\tv_permlane32_swap_b32 %1, %5\n\tv_permlane32_swap_b32 %2, %6\n\tv_permlane32_swap_b32 %3, %7\n\t"
    "v_add_f32 %0, %0, %4\n\tv_add_f32 %2, %2, %6\n\tv_add_f32 %1, %1, %5\n\tv_add_f32 %3, %3, %7\n\t"
    "v_permlane16_swap_b32 %0, %2\n\ts_nop 0\n\tv_permlane16_swap_b32 %1, %3\n\t"
    "v_add_f32 %0, %0, %2\n\tv_add_f32 %1, %1, %3\n\ts_nop 0\n\t"
    DP("row_ror:8") "s_nop 0\n\t" DP("row_ror:4") "s_nop 0\n\t" DP("quad_perm:[2,3,0,1]") "s_nop 0\n\t" DP("quad_perm:[1,0,3,2]") "1:"
    : "+v"(a0), "+v"(a1), "=&v"(a2), "=&v"(a3), "=&v"(a4), "=&v"(a5), "=&v"(a6), "=&v"(a7)
    : "v"(w.x), "v"(w.y), "v"(w.z), "v"(w.w), "v"(s0), "v"(s1), "s"(skip) : "scc");
}
DEV void octsum_store(float v, u64 ep, int wave, rsrc_t r, u32 vo, u32 so) {
  float t;
  asm volatile("s_cmp_lg_u32 %3, 7\n\ts_cbranch_scc1 1f\n\t"
    "v_add_f32_dpp %0, %0, %0 row_ror:8 row_mask:0xf bank_mask:0xf\n\tv_mov_b32 %1, %0\n\tv_lshlrev_b64 v[222:223], 0, %2\n\ts_nop 0\n\tv_permlane16_swap_b32 %0, %1\n\t"
    "v_add_f32 %0, %0, %1\n\tv_mov_b32 %1, %0\n\ts_nop 1\n\tv_permlane32_swap_b32 %0, %1\n\tv_add_f32 %0, %0, %1\n\ts_nop 1\n\t"
    "v_mov_b32_dpp %1, %0 quad_perm:[1,0,3,2] row_mask:0xf bank_mask:0xf\n\tv_cvt_pk_bf16_f32 v220, %0, %1\n\ts_nop 1\n\t"
    "v_mov_b32_dpp v221, v220 quad_perm:[2,3,0,1] row_mask:0xf bank_mask:0xf\n"
    "1:\n\tbuffer_store_dwordx4 v[220:223], %5, %4, %6 offen sc1\n\ts_nop 1"
    : "+v"(v), "=&v"(t) : "s"(ep), "s"(wave), "s"(r), "v"(vo), "s"(so) : "scc", "memory", "v220", "v221", "v222", "v223");
}
template <int SYS> DEV void ld16r(v4u& d, rsrc_t r, u32 v, u32 s) {
  if (SYS) asm volatile("buffer_load_dwordx4 %0, %1, %2, %3 offen sc1" : "+v"(d) : "v"(v), "s"(r), "s"(s) : "memory");
  else asm volatile("buffer_load_dwordx4 %0, %1, %2, %3 offen" : "+v"(d) : "v"(v), "s"(r), "s"(s) : "memory");
}
#define WAIT7(n, a) asm volatile("s_waitcnt vmcnt(" #n ")" : "+v"((a)[0]), "+v"((a)[1]), "+v"((a)[2]), "+v"((a)[3]), "+v"((a)[4]), "+v"((a)[5]), "+v"((a)[6]) :: "memory")
#define WAIT4(n, a) asm volatile("s_waitcnt vmcnt(" #n ")" : "+v"(a[0]), "+v"(a[1]), "+v"(a[2]), "+v"(a[3]) :: "memory")
DEV void dma_rows2(u32 m, u32 v, rsrc_t r, u32 s) {
  asm volatile("s_mov_b32 m0, %0\n\ts_nop 0\n\tbuffer_load_dwordx4 %1, %2, %3 offen lds\n\tbuffer_load_dwordx4 %1, %2, %3 offen offset:896 lds"
               :: "s"(m), "v"(v), "s"(r), "s"(s) : "memory");
}
DEV void dma4k(u32 m, u32 v, rsrc_t r, u32 s) {
  asm volatile("s_mov_b32 m0, %0\n\ts_nop 0\n\tbuffer_load_dwordx4 %1, %2, %3 offen sc1 lds\n\tbuffer_load_dwordx4 %1, %2, %3 offen offset:1024 sc1 lds\n\t"
               "buffer_load_dwordx4 %1, %2, %3 offen offset:2048 sc1 lds\n\tbuffer_load_dwordx4 %1, %2, %3 offen offset:3072 sc1 lds"
               :: "s"(m), "v"(v), "s"(r), "s"(s) : "memory");
}
DEV void touch(u32 m, u32 v, rsrc_t r, u32 s) { asm volatile("s_mov_b32 m0, %0\n\ts_nop 0\n\tbuffer_load_dword %1, %2, %3 offen sc1 lds" :: "s"(m), "v"(v), "s"(r), "s"(s) : "memory"); }
template <int SYS> DEV void dma1k(u32 m, u32 v, rsrc_t r, u32 s) {
  if (SYS) asm volatile("s_mov_b32 m0, %0\n\ts_nop 0\n\tbuffer_load_dwordx4 %1, %2, %3 offen sc1 lds" :: "s"(m), "v"(v), "s"(r), "s"(s) : "memory");
  else asm volatile("s_mov_b32 m0, %0\n\ts_nop 0\n\tbuffer_load_dwordx4 %1, %2, %3 offen lds" :: "s"(m), "v"(v), "s"(r), "s"(s) : "memory");
}
DEV void tr2(u32& a0, u32& a1, u32& b0, u32& b1, u32& c0, u32& c1, u32& d0, u32& d1, u32& t0, u32& t1, u32& u0, u32& u1, u32 odd1, u32 odd2) {
#define CND(d, x, y, q) "v_cndmask_b32_dpp " d ", " x ", " y ", vcc quad_perm:" q " row_mask:0xf bank_mask:0xf\n\t"
#define MOV(d, x, q) "v_mov_b32_dpp " d ", " x " quad_perm:" q " row_mask:0xf bank_mask:0xf\n\t"
#define SEL(d, m, x) "v_bitop3_b32 " d ", " m ", " d ", " x " bitop3:0xca\n\t"
#define Q1 "[1,0,3,2]"
#define Q2 "[2,3,0,1]"
  u32 x0, x1;
  asm volatile("s_mov_b64 vcc, %16\n\ts_nop 0\n\t"
    CND("%8", "%2", "%0", Q1) MOV("%12", "%0", Q1) CND("%9", "%3", "%1", Q1) MOV("%13", "%1", Q1) SEL("%2", "%14", "%12")
    CND("%10", "%6", "%4", Q1) MOV("%12", "%4", Q1) SEL("%3", "%14", "%13") CND("%11", "%7", "%5", Q1) MOV("%13", "%5", Q1) SEL("%6", "%14", "%12")
    "s_mov_b64 vcc, %17\n\t" SEL("%7", "%14", "%13")
    CND("%0", "%10", "%8", Q2) MOV("%12", "%8", Q2) CND("%1", "%11", "%9", Q2) MOV("%13", "%9", Q2) SEL("%10", "%15", "%12")
    CND("%4", "%6", "%2", Q2) MOV("%12", "%2", Q2) SEL("%11", "%15", "%13") CND("%5", "%7", "%3", Q2) MOV("%13", "%3", Q2) SEL("%6", "%15", "%12") SEL("%7", "%15", "%13")
    : "+v"(a0), "+v"(a1), "+v"(b0), "+v"(b1), "+v"(c0), "+v"(c1), "+v"(d0), "+v"(d1), "=&v"(t0), "=&v"(t1), "=&v"(u0), "=&v"(u1), "=&v"(x0), "=&v"(x1)
    : "v"(odd1), "v"(odd2), "s"(0x5555555555555555ull), "s"(0x3333333333333333ull) : "vcc");
}
DEV void tr4(const v4u* g, v4u* f, u32 odd1, u32 odd2) {
  u32 a[4] = {g[0].x, g[0].y, g[0].z, g[0].w}, b[4] = {g[1].x, g[1].y, g[1].z, g[1].w};
  u32 c[4] = {g[2].x, g[2].y, g[2].z, g[2].w}, d[4] = {g[3].x, g[3].y, g[3].z, g[3].w}, t[4], u[4];
  tr2(a[0], a[1], b[0], b[1], c[0], c[1], d[0], d[1], t[0], t[1], u[0], u[1], odd1, odd2);
  tr2(a[2], a[3], b[2], b[3], c[2], c[3], d[2], d[3], t[2], t[3], u[2], u[3], odd1, odd2);
  f[0] = (v4u){a[0], a[1], a[2], a[3]}; f[1] = (v4u){c[0], c[1], c[2], c[3]};
  f[2] = (v4u){u[0], u[1], u[2], u[3]}; f[3] = (v4u){d[0], d[1], d[2], d[3]};
}
typedef short s16x4 __attribute__((ext_vector_type(4)));
DEV v2u ldtr(LAS char* p) { return __builtin_bit_cast(v2u, __builtin_amdgcn_ds_read_tr16_b64_v4i16((LAS s16x4*)p)); }
template <int B> DEV void dotb8(float& a0, float& a1, u32 hx, u32 hy, const v2u* w) {
#define DB(n, x, y) "v_dot2c_f32_bf16_dpp %0, %2, %" #x " row_newbcast:%20+" #n " row_mask:0xf bank_mask:0xf\n\tv_dot2c_f32_bf16_dpp %1, %3, %" #y " row_newbcast:%20+" #n " row_mask:0xf bank_mask:0xf\n\t"
  asm volatile(DB(0, 4, 5) DB(1, 6, 7) DB(2, 8, 9) DB(3, 10, 11) DB(4, 12, 13) DB(5, 14, 15) DB(6, 16, 17) DB(7, 18, 19) : "+v"(a0), "+v"(a1) : "v"(hx), "v"(hy),
    "v"(w[0].x), "v"(w[0].y), "v"(w[1].x), "v"(w[1].y), "v"(w[2].x), "v"(w[2].y), "v"(w[3].x), "v"(w[3].y),
    "v"(w[4].x), "v"(w[4].y), "v"(w[5].x), "v"(w[5].y), "v"(w[6].x), "v"(w[6].y), "v"(w[7].x), "v"(w[7].y), "n"(B));
}
template <int F> DEV void acc8(float* y, v4u p) {
#define A0(d, w, c) "v_dot2_f32_bf16 %" #d ", %" #w ", %" #c ", 0\n\t"
#define A1(d, w, c) "v_dot2_f32_bf16 %" #d ", %" #w ", %" #c ", %" #d "\n\t"
#define A8(A) A(0, 8, 12) A(1, 8, 13) A(2, 9, 12) A(3, 9, 13) A(4, 10, 12) A(5, 10, 13) A(6, 11, 12) A(7, 11, 13)
  if (F) asm volatile(A8(A0) : "=&v"(y[0]), "=&v"(y[1]), "=&v"(y[2]), "=&v"(y[3]), "=&v"(y[4]), "=&v"(y[5]), "=&v"(y[6]), "=&v"(y[7])
                      : "v"(p.x), "v"(p.y), "v"(p.z), "v"(p.w), "s"(0x3f80u), "s"(0x3f800000u));
  else asm volatile(A8(A1) : "+v"(y[0]), "+v"(y[1]), "+v"(y[2]), "+v"(y[3]), "+v"(y[4]), "+v"(y[5]), "+v"(y[6]), "+v"(y[7])
                    : "v"(p.x), "v"(p.y), "v"(p.z), "v"(p.w), "s"(0x3f80u), "s"(0x3f800000u));
}
DEV void lds_set(LAS char* L, u32 off, u32 v) { FENCE(); *(volatile LAS u32*)(L + off) = v; FENCE(); }
DEV void lds_wait4(LAS char* L, u32 off) {
#pragma unroll 1
  for (int it = 0; it < (1 << 22); it++) {
    FENCE();
    const v4u f = *(volatile LAS v4u*)(L + off);
    if (__builtin_amdgcn_readfirstlane(f.x & f.y & f.z & f.w) & 1) break;
  }
  FENCE();
}
#define TY(i)
#define TZ(i)
#define TC(i)
#define TS(i)
#define TN(i)
#define TX(i)

__global__ void __launch_bounds__(512, 2) fused_kernel(const char*, const char*, const char*, const char*, const char*, const char*, const char*,
    const char*, const char*, const char*, const char*, char*, char*) {
  extern __shared__ __attribute__((aligned(16))) char smem[];
  LAS char* L = (LAS char*)smem;
  TS(0); TC(30);
  const char *x, *wre, *wim, *dre, *dim, *wr, *wg, *wu, *wdn, *cv, *wmod; char *out, *ws;
  u32 cpf;
  {
    typedef u64 v4l __attribute__((ext_vector_type(4)));
    v4l k0, k1, k2; u64 k3;
    asm volatile("s_load_dwordx8 %0, %5, 0x0\n\ts_load_dwordx8 %1, %5, 0x20\n\ts_load_dwordx8 %2, %5, 0x40\n\ts_load_dwordx2 %3, %5, 0x60\n\t"
      "s_getpc_b64 s[4:5]\n.Lfk_pc:\n\ts_mov_b32 s6, .Lfk_end-.Lfk_pc\n\ts_mov_b32 s7, 0x20000\n\t"
      "v_xor_b32 %4, %6, %7\n\tv_cmp_gt_u32 vcc, 8, %4\n\tv_lshlrev_b32 %4, 7, %7\n\tv_cndmask_b32 %4, -1, %4, vcc\n\t"
      "buffer_load_dword %4, %4, s[4:7], 0 offen\n\t"
      "s_waitcnt lgkmcnt(0)"
      : "=&s"(k0), "=&s"(k1), "=&s"(k2), "=&s"(k3), "=&v"(cpf) : "s"(__builtin_amdgcn_kernarg_segment_ptr()), "s"(blockIdx.x), "v"(threadIdx.x)
      : "memory", "vcc", "s4", "s5", "s6", "s7");
    x = (const char*)k0[0]; wre = (const char*)k0[1]; wim = (const char*)k0[2]; dre = (const char*)k0[3];
    dim = (const char*)k1[0]; wr = (const char*)k1[1]; wg = (const char*)k1[2]; wu = (const char*)k1[3];
    wdn = (const char*)k2[0]; cv = (const char*)k2[1]; wmod = (const char*)k2[2]; out = (char*)k2[3]; ws = (char*)k3;
  }
#define CPFUSE asm volatile("" :: "v"(cpf))
  const int b = blockIdx.x, xcd = b & 7, r = b >> 3, j = r & 3;
  const int wave = __builtin_amdgcn_readfirstlane(threadIdx.x >> 6), lane = threadIdx.x & 63;
  const int tw = wave & 3, kw = wave >> 2;
  const int kq = xcd & 3, tq = (xcd >> 2) * 8 + (r >> 2);
  const int hp = xcd & 1, sp = (xcd >> 1) * 8 + (r >> 2);
  const bool comp = kw == (j >> 1);
  asm volatile("" :: "s"(x), "s"(wre), "s"(wim), "s"(dre), "s"(dim), "s"(wr), "s"(wg), "s"(wu), "s"(wdn), "s"(cv), "s"(wmod), "s"(out), "s"(ws));
  const rsrc_t rws = mk(ws, WS_SIZE);
  v2u epv; ldepa(epv, rws);
  u64 ep;
#define GETEP() { CPFUSE; u32 e0, e1; asm volatile("v_readfirstlane_b32 %0, %2\n\tv_readfirstlane_b32 %1, %3" : "=s"(e0), "=s"(e1) : "v"(epv.x), "v"(epv.y) : "memory"); \
                  ep = (((u64)e1 << 32) | e0) * 6364136223846793005ull + 1442695040888963407ull; }
  const int q = lane >> 4, l15 = lane & 15;

  const rsrc_t rd = mk(hp ? dim : dre, D * D * 2);
  const int mt = xcd * 14 + (r >> 2) * 2 + (((j ^ (r >> 2)) >> 1) & 1), mh = mt >= 28, ct = mt - mh * 28;
  const bool crit = xcd < 4 && r < 28 && ((j ^ (r >> 2)) & 1) == 0;
  auto mod_load = [&](v4u* mw, u32* mc) {
    const rsrc_t rm = mk(wmod, D * 6 * D * 2), rc = mk(cv, D * 2);
    const u32 row = mh * 224 + (lane >> 2) * 2, vo = row * 5376 + (lane & 3) * 16;
#pragma unroll
    for (int i = 0; i < 7; i++) ld4a(mc[i], rc, row * 2, i * 64);
#pragma unroll
    for (int i = 0; i < 14; i++) ld16a<0>(mw[i], rm, vo, ct * 64 + ((i >> 1) * 32 + (i & 1)) * 5376);
  };
  auto mod_silu = [&](u32* mc) {
#pragma unroll
    for (int i = 0; i < 7; i++) { const float c0 = lo(mc[i]), c1 = hi(mc[i]); mc[i] = pk(c0 * sigm(c0), c1 * sigm(c1)); }
  };
  auto mod_finish = [&](v4u* mw, u32* mc) {
    float a[8] = {0, 0, 0, 0, 0, 0, 0, 0};
#pragma unroll
    for (int i = 0; i < 7; i++)
#pragma unroll
      for (int k = 0; k < 4; k++) {
        const u32 w0 = mw[2 * i][k], w1 = mw[2 * i + 1][k];
        a[2 * k] = __builtin_amdgcn_fdot2_f32_bf16(bf2(__builtin_amdgcn_perm(w1, w0, 0x05040100u)), bf2(mc[i]), a[2 * k], false);
        a[2 * k + 1] = __builtin_amdgcn_fdot2_f32_bf16(bf2(__builtin_amdgcn_perm(w1, w0, 0x07060302u)), bf2(mc[i]), a[2 * k + 1], false);
      }
#pragma unroll
    for (int k = 0; k < 4; k++) a[k] = sum32(a[k], a[k + 4]);
#pragma unroll
    for (int k = 0; k < 2; k++) a[k] = sum16(a[k], a[k + 2]);
#pragma unroll
    for (int k = 0; k < 2; k++) { a[k] += dpp<ROR8>(a[k]); a[k] += dpp<ROR4>(a[k]); }
    const u32 so = WS_MODG + mh * 7168 + ct * 256, cp = (lane >> 2) & 3;
    const u32 vs = cp < MREP ? ((lane & 3) * 4 + (lane >> 5) * 2 + ((lane >> 4) & 1)) * 16 + cp * RSTR : OOB;
    st16((v4u){fu(a[0]), fu(a[1]), (u32)ep, (u32)(ep >> 32)}, rws, vs, so);
    if (MREP == 8) st16((v4u){fu(a[0]), fu(a[1]), (u32)ep, (u32)(ep >> 32)}, rws, vs, so + 4 * RSTR);
  };
  const u32 go = lane < 56 ? (tw * 56 + lane) * 16 : OOB;
  auto g_load = [&](v4u* g) {
#pragma unroll
    for (int p = 0; p < 4; p++) ld16a<1>(g[p], rws, go, WS_MODG + (xcd * MREP >> 3) * RSTR + (p & 1) * 7168 + (p >> 1) * 3584);
  };
  auto g_put = [&](const v4u* g) {
    const u32 sh = pk(uf(g[0].x) + uf(g[1].x), uf(g[0].y) + uf(g[1].y)), sc = pk(uf(g[2].x) + uf(g[3].x), uf(g[2].y) + uf(g[3].y));
    const u32 c1 = pk(1.f + lo(sc), 1.f + hi(sc));
    if (lane < 56) {
      *(LAS f32x2*)(L + L_MP + (tw * 56 + lane) * 8) = (f32x2){lo(sh), hi(sh)};
      *(LAS f32x2*)(L + L_MP + 1792 + (tw * 56 + lane) * 8) = (f32x2){lo(c1), hi(c1)};
    }
    lds_set(L, L_CNT + 16 + tw * 4, 1);
  };
  const int rot = ROT ? (r >> 2) & 3 : 0;
  v4u G[8][4];
  auto wload = [&](int s) {
    const rsrc_t rw = mk((kq & 1) ? wim : wre, S * S * 2);
    const u32 vo = ((lane >> 2) & 3) * 4 * 4096 + (q * 4 + (lane & 3)) * 16;
    const u32 so = (tq * 128 + tw * 32) * 4096 + (kq >> 1) * 2048 + kw * 1024;
    const u32 f0 = (kw ^ (j & 1)) * 16 * 4096;
    constexpr int OL[8] = {ORD_L};
    const int g = OL[s];
#pragma unroll
    for (int i = 0; i < 4; i++) ld16a<0>(G[g][i], rw, vo, so + ((g + rot) & 3) * 256 + (f0 ^ ((g >> 2) * 16 * 4096)) + i * 4096);
    SCHED();
  };
  if (threadIdx.x < 16) *(LAS u32*)(L + L_CNT + threadIdx.x * 4) = 0;
  WAITLGKM0();
  if (comp) {
    barrier();
    __builtin_amdgcn_s_setprio(2);
    const rsrc_t rx = mk(x, S * D * 2);
    if (lane < 56) {
      for (int i = 0; i < 8; i++) {
        int p = (i + j * 2) & 7;
        dma_rows2(L_X + tw * 14336 + p * 1792, (lane ^ p) * 16, rx, (sp * 64 + tw * 16 + p * 2) * 896);
      }
    }
    TX(1); WAITVM(0); GETEP();
    const u32 sw0 = l15 * 896 + ((q ^ (l15 >> 1)) << 4), sw1 = l15 * 896 + (((4 + q) ^ (l15 >> 1)) << 4);
    float xn[112];
    {
      v4u t[14];
#pragma unroll
      for (int kb = 0; kb < 14; kb++) t[kb] = *(LAS v4u*)(L + L_X + tw * 14336 + ((kb & 1) ? sw1 : sw0) + (kb >> 1) * 128);
      const v4u ones = {0x3f803f80u, 0x3f803f80u, 0x3f803f80u, 0x3f803f80u};
      f32x4 sm = {0, 0, 0, 0};
#pragma unroll
      for (int kb = 0; kb < 14; kb++) sm = mfma(ones, t[kb], sm);
      const float m = sm[0] * (1.f / D);
      f32x2 v[56], a0 = {0, 0}, a1 = {0, 0};
#pragma unroll
      for (int i = 0; i < 56; i += 2) {
        v[i] = (f32x2){lo(t[i >> 2][i & 3]), hi(t[i >> 2][i & 3])} - m; v[i + 1] = (f32x2){lo(t[i >> 2][(i & 3) + 1]), hi(t[i >> 2][(i & 3) + 1])} - m;
        a0 += v[i] * v[i]; a1 += v[i + 1] * v[i + 1];
      }
      a0 += a1;
      float var = a0.x + a0.y; var = sum16(var, var); var = sum32(var, var);
      const float rs = __builtin_amdgcn_rsqf(var * (1.f / D) + 1e-5f);
#pragma unroll
      for (int i = 0; i < 56; i++) { f32x2 d = v[i] * rs; xn[2 * i] = d.x; xn[2 * i + 1] = d.y; }
    }
#pragma unroll
    for (int i = 0; i < 112; i++) asm volatile("" : "+v"(xn[i]));
    TX(2);
    u32 wa[2][2] = {{sw0, sw0 + 57344}, {sw1, sw1 + 57344}}, ma = L_MP + q * 32, ca = L_MP + 1792 + (l15 >> 3) * 128 + q * 32 + (l15 & 7) * 4;
    asm volatile("" : "+v"(wa[0][0]), "+v"(wa[0][1]), "+v"(wa[1][0]), "+v"(wa[1][1]), "+v"(ma), "+v"(ca));
    if (GSY == 2) barrier();
    lds_wait4(L, L_CNT + 16);
    TX(4);
    f32x4 acc[7];
    {
      v4u wf[2][7], mp[2][2], cp[2][2], hf[2];
      float cs[7];
#pragma unroll
      for (int k = 0; k < 2; k++) {
        mp[k][0] = *(LAS v4u*)(L + ma + k * 128); mp[k][1] = *(LAS v4u*)(L + ma + k * 128 + 16);
        if (NDP) { cp[k][0] = *(LAS v4u*)(L + ma + 1792 + k * 128); cp[k][1] = *(LAS v4u*)(L + ma + 1792 + k * 128 + 16); }
        else if (k == 0) { cs[0] = *(LAS float*)(L + ca); cs[1] = *(LAS float*)(L + ca + 256); }
        SCHED();
#pragma unroll
        for (int et = 0; et < 7; et++) wf[k][et] = *(LAS v4u*)(L + wa[k][et >> 2] + (et & 3) * 14336);
        SCHED();
      }
#pragma unroll
      for (int g = 2; g < 7; g++) if (!NDP) cs[g] = *(LAS float*)(L + ca + g * 256);
      SCHED();
      u32 h[8];
#define FM(kb, i, m) { h[i] = m[(i) >> 2][(i) & 3]; if (NDP) asm volatile("v_fmac_f32 %0, %1, %2" : "+v"(h[i]) : "v"(cp[(kb) & 1][(i) >> 2][(i) & 3]), "v"(xn[(kb) * 8 + (i)])); \
                       else fmacb<((kb) & 1) * 8 + (i)>(h[i], cs[(kb) >> 1], xn[(kb) * 8 + (i)]); }
      FM(0, 0, mp[0]) FM(0, 1, mp[0]) FM(0, 2, mp[0]) FM(0, 3, mp[0]) FM(0, 4, mp[0]) FM(0, 5, mp[0]) FM(0, 6, mp[0]) FM(0, 7, mp[0])
      hf[0] = (v4u){pk(uf(h[0]), uf(h[1])), pk(uf(h[2]), uf(h[3])), pk(uf(h[4]), uf(h[5])), pk(uf(h[6]), uf(h[7]))};
      asm volatile("s_nop 1" : "+v"(hf[0]));
      SCHED();
      auto step = [&](auto KB) {
        constexpr int kb = KB.value, c = kb & 1, nx = c ^ 1;
#pragma unroll
        for (int et = 0; et < 7; et++) {
          if (kb == 0) mma0(acc[et], hf[c], wf[c][et]); else mma(acc[et], hf[c], wf[c][et]);
          SCHED();
          if (kb < 12) {
            wf[c][et] = *(LAS v4u*)(L + wa[c][et >> 2] + (et & 3) * 14336 + ((kb + 2) >> 1) * 128);
            if (et < 2) mp[c][et] = *(LAS v4u*)(L + ma + (kb + 2) * 128 + et * 16);
            if (NDP && et >= 2 && et < 4) cp[c][et - 2] = *(LAS v4u*)(L + ma + 1792 + (kb + 2) * 128 + (et - 2) * 16);
          }
          SCHED();
          if constexpr (kb < 13) {
            if (et == 0) { FM(kb + 1, 0, mp[nx]) FM(kb + 1, 1, mp[nx]) }
            if (et == 1) { FM(kb + 1, 2, mp[nx]) FM(kb + 1, 3, mp[nx]) }
            if (et == 2) { FM(kb + 1, 4, mp[nx]) FM(kb + 1, 5, mp[nx]) }
            if (et == 3) { FM(kb + 1, 6, mp[nx]) FM(kb + 1, 7, mp[nx]) }
            if (et == 4) { hf[nx].x = pk(uf(h[0]), uf(h[1])); hf[nx].y = pk(uf(h[2]), uf(h[3])); }
            if (et == 5) { hf[nx].z = pk(uf(h[4]), uf(h[5])); hf[nx].w = pk(uf(h[6]), uf(h[7])); }
          }
          SCHED();
        }
      };
      sfor<14>(step);
      asm volatile("s_nop 7" : "+v"(acc[0]), "+v"(acc[1]), "+v"(acc[2]), "+v"(acc[3]), "+v"(acc[4]), "+v"(acc[5]), "+v"(acc[6]));
    }
    TX(5);
    {
      const u32 sg = hp ? 0x80008000u : 0u;
      const u32 vo = ((q & 1) * 32 + (tw & 1) * 2 + (q >> 1)) * 1024 + (((sp & 1) * 2 + (tw >> 1)) * 16 + l15) * 16;
      const u32 so = WS_AB + ((((xcd >> 2) * 2 + hp) * 4 + j) * 7 * 32 + ((sp & 15) >> 1) * 4) * 1024;
#pragma unroll
      for (int et = 0; et < 7; et += 2) {
        const int e1 = et < 6 ? et + 1 : et;
        auto a = __builtin_amdgcn_permlane16_swap(pk(acc[et][0], acc[et][1]) ^ sg, pk(acc[e1][0], acc[e1][1]) ^ sg, false, false);
        auto c = __builtin_amdgcn_permlane16_swap(pk(acc[et][2], acc[et][3]) ^ sg, pk(acc[e1][2], acc[e1][3]) ^ sg, false, false);
        st16((v4u){a[0], c[0], a[1], c[1]}, rws, (et < 6 || !(q & 1)) ? vo : OOB, so + et * 32768);
      }
    }
#pragma unroll
    for (int g = 0; g < PREW; g++) wload(g);
  } else {
    int p0 = tw * 14, np = 14;
    constexpr int NP1 = (56 - NP0) / 3;
    if (crit) { p0 = tw ? NP0 + (tw - 1) * NP1 : 0; np = tw ? NP1 : NP0; }
    if (crit && tw == 0) {
      v4u mw[14]; u32 mc[7];
      __builtin_amdgcn_s_setprio(3);
      mod_load(mw, mc);
      TX(18);
      barrier();
      WAIT7(14, mc);
      GETEP();
      mod_silu(mc);
      asm volatile("" : "+v"(mc[0]), "+v"(mc[1]), "+v"(mc[2]), "+v"(mc[3]), "+v"(mc[4]), "+v"(mc[5]), "+v"(mc[6]));
      WAIT7(7, mw); WAIT7(0, mw + 7);
      mod_finish(mw, mc);
      TX(20);
      __builtin_amdgcn_s_setprio(1);
    } else barrier();
    if (TCB && tw == 3) touch(L_JUNK, 0, rws, WS_BF + ((((xcd >> 2) * 2 + hp) * 4 + j) * 16 + (sp & 15)) * 128);
#pragma unroll
    for (int g = 0; g < PREW; g++) wload(g);
    if (lane < 56) {
      int p = p0 + (((r >> 2) * np) >> 3);
#pragma unroll 1
      for (int i = 0; i < np; i++) {
        dma_rows2(L_WD + p * 1792, (lane ^ (p & 7)) * 16, rd, (j * 112 + p * 2) * 896);
        if (++p == p0 + np) p = p0;
      }
    }
    __builtin_amdgcn_s_setprio(0);
    if (GEP) {
      if (crit) asm volatile("s_waitcnt vmcnt(%1)" : "+v"(epv) : "n"(NP1 * 2) : "memory"); else asm volatile("s_waitcnt vmcnt(28)" : "+v"(epv) :: "memory");
      GETEP();
      asm volatile("" : "+s"(ep));
    }
    TX(1);
  }

  v4u A[2][16];
  u32 sc0, sc1;
  {
    const u32 odd1 = -(lane & 1), odd2 = -((lane >> 1) & 1);
    auto wtr = [&](auto Si, auto Ni) {
      constexpr int OL[8] = {ORD_L}; constexpr int g = OL[Si.value];
      asm volatile("s_waitcnt vmcnt(%4)" : "+v"(G[g][0]), "+v"(G[g][1]), "+v"(G[g][2]), "+v"(G[g][3]) : "n"(Ni.value) : "memory");
      tr4(G[g], &A[g >> 2][(g & 3) * 4], odd1, odd2); SCHED();
    };
    v4u gg[4];
    auto gather = [&]() {
      TX(3);
      if (EARLY == 0) WAIT4(0, gg); else if (EARLY == 1) WAIT4(4, gg); else WAIT4(8, gg);
      TX(5);
      if (!GEP) GETEP();
      if (!clean<56>(bad4(gg, ep))) {
        _Pragma("unroll 1") for (int it = 0; it < SPIN; it++) { g_load(gg); WAIT4(0, gg); if (clean<56>(bad4(gg, ep))) break; }
      }
      g_put(gg);
      if (PRX >= 2) __builtin_amdgcn_s_setprio(1);
      TX(4);
    };
    auto drain = [&]() {
      asm volatile("s_waitcnt vmcnt(%0)" :: "n"(NPRE * 4) : "memory");
      __builtin_amdgcn_raw_buffer_store_b64((v2u){(u32)ep, (u32)(ep >> 32)}, rws, lane == 0 ? 0 : OOB,
                                            WS_BF + ((((xcd >> 2) * 2 + hp) * 4 + j) * 16 + (sp & 15)) * 128 + tw * 16, SC1);
      if (WFLAG == 2) lds_set(L, L_CNT + 32 + tw * 4, 1);
      if (PRX) __builtin_amdgcn_s_setprio(0);
      TX(6);
    };
    if (!comp) {
      g_load(gg);
      if (GSY == 1) { lds_set(L, L_CNT + tw * 4, 1); lds_wait4(L, L_CNT); }
      if (GSY == 2) barrier();
      if (EARLY == 0) gather();
    }
    u32 fla = L_CNT + 32 + tw * 4;
    asm volatile("" : "+v"(fla));
    sfor<8 - PREW>([&](auto Gi) {
      constexpr int g = Gi.value + PREW;
      if (g == NPRE && comp) drain();
      u32 fl;
      if (UNT && g >= UNT && g < 7) asm volatile("ds_read_b32 %0, %1" : "=v"(fl) : "v"(fla) : "memory");
      wload(g);
      if (!comp) {
        if (g + 1 == EARLY + PREW) gather();
        if (UNT && g >= UNT && g < 7) {
          u32 t;
          asm volatile("s_waitcnt lgkmcnt(0)\n\tv_readfirstlane_b32 %0, %1\n\ts_cmp_lg_u32 %0, 0\n\ts_cbranch_scc1 1f\n\ts_waitcnt vmcnt(%2)\n1:" : "=&s"(t) : "v"(fl), "n"(4) : "scc", "memory");
        } else if (!ILV && g < 7) { constexpr int LV[8] = {LAV}; if (g >= LV[g]) asm volatile("s_waitcnt vmcnt(%0)" :: "n"(LV[g] * 4) : "memory"); }
      } else if (UNT && g >= UNT && g < 7) asm volatile("s_waitcnt lgkmcnt(0)" :: "v"(fl) : "memory");
      if constexpr (ILV && g >= ILV && g < 7) wtr(IC<g - ILV>{}, IC<ILV * 4>{});
    });
    u32 lc;
    ld2a(lc, mk(cv, D * 2), lane * 2, wave * 128);
    if (PRX == 3 && comp) __builtin_amdgcn_s_setprio(2);
    TX(19);
    sfor<ILV ? ILV + 1 : 8>([&](auto Gi) {
      constexpr int g = Gi.value + (ILV ? 7 - ILV : 0);
      wtr(IC<g>{}, IC<(7 - g) * 4 + 1>{});
    });
    asm volatile("s_waitcnt vmcnt(0)" : "+v"(lc) :: "memory");
    sc0 = pk(lo(lc) * sigm(lo(lc)), 0.f); sc1 = sc0 << 16;
    asm volatile("" : "+v"(sc0), "+v"(sc1));
  }
  __builtin_amdgcn_s_setprio(0);
  TS(7); TC(31);
  const u32 lane16 = lane * 16;
  const int o_self = kq * 4 + j;
  const int mi = xcd * 7 + r;
  const int pid = (r * 8 + wave) * 2;
  u32 cm_vo = (tw * 4 + (kw ^ (j & 1)) * 2 + (l15 >> 3)) * 28672 + q * 64 + (lane & 7) * 8, cm_so = WS_CM + tq * 16 * 28672 + o_self * 7 * 256;
  u32 mb_so = WS_CM + (tq * 16 + o_self) * 28672 + wave * 1024;
  u32 wp_vo = r < 28 ? (lane >> 5) * 5376 + (lane & 31) * 16 : OOB, wp_so = (r * 16 + wave * 2) * 5376 + ((1792 + xcd * 448) & ~127);
  u32 tc_vo = wave == 0 ? (lane < 14 ? WS_DH + b * 2048 + lane * 128 : lane < 42 ? WS_DB + b * 4096 + (lane - 14) * 128 : lane == 42 ? WS_DF + b * 128 :
                           lane < 46 ? WS_DG + b * DGS + (lane - 43) * DRP : lane < 49 ? WS_CF + (tq * 16 + o_self) * 128 + (lane - 46) * CRP : OOB)
            : wave == 1 && r < 7 && lane < 28 ? WS_EG + mi * 3584 + lane * 128 : OOB;
  u32 mlp_vo = (lane >> 2) * 4 * 3584 + (lane & 3) * 16, mlp_so = wave * 64 * 3584 + mi * 64;
  int mlp_on = __builtin_amdgcn_readfirstlane(r < 7 && wave < 7);
  asm volatile("" : "+s"(mlp_on));
  u32 lw_vo = r < 28 && wave < 7 ? lane * 5376 : OOB, lw_so = wave * 64 * 5376 + 1792 + xcd * 448 + r * 16;
  u32 pf_vo = pid < 448 ? (lane >> 5) * 3584 + (lane & 31) * 16 : OOB, pf_so = (pid >= 224 ? pid - 224 : pid) * 7168 + ((xcd * 448) & ~127);
  u32 pd_vo = r * 8 + wave < 196 ? lane16 : OOB, pd_so = xcd * 224 * 896 + (r * 8 + wave) * 1024;
  u32 ms_vo = wave == 7 && r < 28 && (lane & 59) == 0 ? lane * 4 : OOB, ms_so = WS_MODL + (xcd * 28 + r) * 32;
  u32 w7_vo = wave < 7 ? lane16 : OOB, l56_vo = lane < 56 ? lane16 : OOB, xr_so = (tq * 128 + o_self * 8 + wave) * 896;
  u32 dsrc = WS_AB + o_self * 7 * 32768 + kw * 16384 + ((tw + rot) & 3) * 4096, wave4k = wave * 4096, gm_so = WS_MODL + wave * 1024;
  LAS char *mp_wa = L + L_MPART + wave * 32 + q * 8, *mp_ra = L + L_MPART + lane * 4;
  LAS char *rb0 = L + kw * 16384 + lane16, *rb2 = rb0 + 65536, *red_w = L + L_RED + wave * 1024 + lane16, *red_r = L + L_RED + (wave ^ 4) * 1024 + lane16;
  asm volatile("" : "+v"(mp_ra), "+v"(rb0), "+v"(rb2), "+v"(red_w), "+v"(red_r));
  u32 bp_so = WS_AB + (o_self * 7 + 3 + (wave >> 1)) * 32768 + (r >> 2) * 4096 + (wave & 1) * 2048;
  asm volatile("" : "+s"(bp_so));
  rsrc_t r_mod = mk(wmod, D * 6 * D * 2), r_pf = mk(pid >= 224 ? wu : wg, D * F * 2), r_g = mk(wg, D * F * 2), r_u = mk(wu, D * F * 2), r_dn = mk(wdn, D * F * 2);
  rsrc_t r_x = mk(x, S * D * 2), r_wr = mk(wr, D * 2);
  asm volatile("" : "+v"(cm_vo), "+v"(wp_vo), "+v"(tc_vo), "+v"(mlp_vo), "+v"(lw_vo), "+v"(pf_vo), "+v"(pd_vo), "+v"(ms_vo), "+v"(w7_vo), "+v"(l56_vo), "+v"(mp_wa));
  asm volatile("" : "+s"(cm_so), "+s"(mb_so), "+s"(mlp_so), "+s"(lw_so), "+s"(pf_so), "+s"(pd_so), "+s"(ms_so), "+s"(xr_so), "+s"(dsrc), "+s"(wave4k), "+s"(gm_so), "+s"(wp_so));
  asm volatile("" : "+s"(r_mod), "+s"(r_pf), "+s"(r_g), "+s"(r_u), "+s"(r_dn), "+s"(r_x), "+s"(r_wr));
  if (!comp) {
    const u32 bsrc = WS_AB + o_self * 7 * 32768 + tw * 8192;
    lds_wait4(L, L_CNT + 32);
    const u32 fo = lane < 16 ? WS_BF + (o_self * 16 + tw * 4 + (lane >> 2)) * 128 + (lane & 3) * 16 : OOB;
    v2u f0, f1, f2;
#define BFL(c, f) asm volatile("buffer_load_dwordx2 %0, %1, %2, 0 offen sc1" : c(f) : "v"(fo), "s"(rws) : "memory")
#define BFS(f) asm volatile("s_waitcnt vmcnt(%1)" : "+v"(f) : "n"(BPD - 1) : "memory"); if (clean<16>(bad(f, ep))) break; BFL("+v", f);
    BFL("=&v", f0);
    if (BPD > 1) { __builtin_amdgcn_s_sleep(BSL); BFL("=&v", f1); }
    if (BPD > 2) { __builtin_amdgcn_s_sleep(BSL); BFL("=&v", f2); }
    _Pragma("unroll 1") for (int it = 0; it < SPIN; it++) {
      BFS(f0)
      if (BPD > 1) { BFS(f1) }
      if (BPD > 2) { BFS(f2) }
    }
    TX(2);
    const u32 d0 = (tw >> 1) * 16384 + ((tw * 2 - rot) & 3) * 4096, d1 = (tw >> 1) * 16384 + ((tw * 2 + 1 - rot) & 3) * 4096;
#pragma unroll
    for (int n = 0; n < (S2L ? 2 : 3); n++) { dma4k(n * 32768 + d0, lane16, rws, bsrc + n * 32768); dma4k(n * 32768 + d1, lane16, rws, bsrc + n * 32768 + 4096); }
#pragma unroll
    for (int n = 3; n < 7; n++) dma1k<1>(L_JUNK, lane16, rws, bsrc + n * 32768 + (r >> 2) * 1024);
    WAITVM(20);
    asm volatile("" :: "v"(f0));
    if (BPD > 1) asm volatile("" :: "v"(f1));
    if (BPD > 2) asm volatile("" :: "v"(f2));
  }
  WAITLGKM0();
  barrier();
  TS(8); TC(16);

  v4u xr, wrr, gm;
  {
    auto bfrag = [&](int n, int k) { return *(LAS v4u*)(((n & 2) ? rb2 : rb0) + (n & 1) * 32768 + k * 1024); };
    v4u B[4];
#pragma unroll
    for (int k = 0; k < 4; k++) B[k] = bfrag(0, k);
    f32x4 a0[2], a1[2], pr;
    constexpr int PS[4] = {PFS};
    float mv, ma[2]; v4u lw;
    auto part = [&](f32x4 a, int n) {
      __builtin_amdgcn_raw_buffer_store_b64((v2u){pk(vadd(pr[0], a[0]), vadd(pr[1], a[1])), pk(vadd(pr[2], a[2]), vadd(pr[3], a[3]))}, rws, cm_vo, cm_so + n * 256, SC1);
    };
#pragma unroll
    for (int n = 0; n < 7; n++) {
      const int c = n & 1;
#pragma unroll
      for (int k = 0; k < 16; k++) {
        if (k == 12) {
          const int X = (n == PS[0] || n == PS[0] + 1) + (n == PS[1] || n == PS[1] + 1) + (n == PS[2] || n == PS[2] + 1) + (n == PS[3] || n == PS[3] + 1);
          if (n < 6) asm volatile("s_waitcnt vmcnt(%0)" :: "n"((n == 0 ? 13 : n == 1 ? 10 : n == 2 ? 5 : n == 3 ? 5 : n == 4 ? 6 : 1) + X) : "memory");
          barrier();
          if (n > 0) pr = *(LAS f32x4*)(red_r + (c ^ 1) * 8192);
        }
        if (k == 0) { mma0(a0[c], B[0], A[0][0]); mma0(a1[c], B[0], A[1][0]); }
        else { mma(a0[c], B[k & 3], A[0][k]); mma(a1[c], B[k & 3], A[1][k]); }
        SCHED();
        if (n < 6 || k < 12) B[k & 3] = k < 12 ? bfrag(n, k + 4) : bfrag(n + 1, k - 12);
        if (k == 2 && n > 0) { asm volatile("" : "+v"(a1[c ^ 1])); *(LAS f32x4*)(red_w + (c ^ 1) * 8192) = a1[c ^ 1]; }
        if (k == 14 && n > 0) part(a0[c ^ 1], n - 1);
        if ((k == 13 || k == 15) && n < 4) dma1k<1>(((n + 3) & 3) * 32768 + wave4k + (k - 13) * 512, lane16, rws, dsrc + (n + 3) * 32768 + (k - 13) * 512);
        if ((k == 1 || k == 3) && n >= 1 && n < 5) dma1k<1>(((n + 2) & 3) * 32768 + wave4k + (k + 3) * 512, lane16, rws, dsrc + (n + 2) * 32768 + (k + 3) * 512);
        if (n == PS[0] && k == 5) dma1k<0>(L_JUNK, pf_vo, r_pf, pf_so);
        if (n == PS[1] && k == 7) dma1k<0>(L_JUNK, pf_vo, r_pf, pf_so + 7168);
        if (n == PS[2] && k == 9) dma1k<0>(L_JUNK, pd_vo, r_dn, pd_so);
        if (n == PS[3] && k == 11) touch(L_JUNK, tc_vo, rws, 0);
        if (n == 5 && k == 13) { ld16a<0>(xr, r_x, l56_vo, xr_so); ld16a<0>(wrr, r_wr, lane16, 0); }
        const int t = n * 16 + k;
        if (S2L && n == 0 && k < 8 && (k & 1)) dma1k<1>(65536 + wave4k + (k - 1) * 512, lane16, rws, dsrc + 65536 + (k - 1) * 512);
        if (S2L && n == 0 && (k == 9 || k == 11)) dma1k<1>(L_JUNK, lane16, rws, bp_so + (k - 9) * 512);
        if (t == (S2L ? 0 : 1)) dma1k<0>(L_JUNK, wp_vo, r_mod, wp_so);
        if (t == 16) ld16a<0>(lw, r_mod, lw_vo, lw_so);
        if (t == 45) asm volatile("" : "+v"(lw), "=v"(ma[0]), "=v"(ma[1]));
        if (t == TA0) colsum8(ma[0], ma[1], lw, sc0, sc1, TA0 == TA1 ? 0 : kw);
        if (t == TA1 && TA0 != TA1) colsum8(ma[0], ma[1], lw, sc0, sc1, kw ^ 1);
        if (t == TA1 + 1) *(LAS f32x2*)mp_wa = (f32x2){ma[0], ma[1]};
        if (t == 61) mv = *(LAS float*)mp_ra;
        if (t == TB) octsum_store(mv, ep, wave, rws, ms_vo, ms_so);
        if (t == 109) ld16a<1>(gm, rws, w7_vo, gm_so);
        SCHED();
      }
    }
    TS(9); TC(17);
    asm volatile("s_nop 7" : "+v"(a0[0]), "+v"(a1[0]));
    *(LAS f32x4*)red_w = a1[0];
    WAITLGKM0(); barrier();
    pr = *(LAS f32x4*)red_r;
    part(a0[0], 6);
    TY(21);
  }
  v2u cf = {0, 0}, cf1, cf2, epv2 = {(u32)ep, (u32)(ep >> 32)};
  u32 cf_vo = lane < 16 ? WS_CF + (tq * 16 + lane) * 128 : OOB, l0_vo = lane == 0 ? 0 : OOB, c3_vo = lane < CPD ? lane * CRP : OOB, cf_so = WS_CF + (tq * 16 + o_self) * 128;
  asm volatile("" : "+s"(cf_so));
  asm volatile("s_waitcnt vmcnt(0)" : "+v"(gm), "+v"(xr), "+v"(wrr), "+v"(cf), "+v"(epv2), "+v"(cf_vo), "+v"(l0_vo), "+v"(c3_vo) :: "memory");
  TY(22); TZ(29);
  auto mrput = [&]() {
    if (wave < 7) {
      if (!clean<64>(bad(gm, ep))) {
        _Pragma("unroll 1") for (int it = 0; it < SPIN; it++) {
          ld16r<1>(gm, rws, lane16, gm_so);
          asm volatile("s_waitcnt vmcnt(0)" : "+v"(gm) :: "memory");
          if (clean<64>(bad(gm, ep))) break;
        }
      }
      *(LAS f32x4*)(L + L_MR + wave * 1024 + lane16) = (f32x4){lo(gm.x), hi(gm.x), lo(gm.y), hi(gm.y)};
    }
    WAITLGKM0();
  };
  if (wave == 7) __builtin_amdgcn_s_setprio(3);
  if (MRB) mrput();
  barrier();
  TY(23);
  asm volatile("s_cmp_lg_u32 %6, 7\n\ts_cbranch_scc1 1f\n\tbuffer_store_dwordx2 %3, %4, %2, %5 offen sc1\n\tbuffer_load_dwordx2 %0, %1, %2, 0 offen sc1\n1:"
               : "+v"(cf) : "v"(cf_vo), "s"(rws), "v"(epv2), "v"(c3_vo), "s"(cf_so), "s"(wave) : "scc", "memory");
  auto cfload = [&](v2u& c, u32 o) { asm volatile("s_cmp_lg_u32 %3, 7\n\ts_cbranch_scc1 1f\n\tbuffer_load_dwordx2 %0, %1, %2, %4 offen sc1\n1:" : "=&v"(c) : "v"(cf_vo), "s"(rws), "s"(wave), "s"(o) : "scc", "memory"); };
  auto gload = [&](int i) {
    asm volatile("s_cmp_eq_u32 %6, 0\n\ts_cbranch_scc1 1f\n\ts_mov_b32 m0, %0\n\ts_nop 0\n\tbuffer_load_dwordx4 %2, %3, %4 offen lds\n\ts_mov_b32 m0, %1\n\ts_nop 0\n\tbuffer_load_dwordx4 %2, %3, %5 offen lds\n1:"
      :: "s"(L_G + wave * 4352 + i * 1088), "s"(L_G + wave * 4352 + i * 1088 + 1088), "v"(mlp_vo), "s"(r_g), "s"(mlp_so + i * 3584), "s"(mlp_so + i * 3584 + 3584), "s"(mlp_on) : "scc", "memory");
  };
  if (!MRB) { mrput(); barrier(); }
  if (GSP >= 1) gload(0);
  if (GSP == 1) gload(2);
  TS(10);
  const bool act = lane < 56;
  f32x2 ga[4], sb[4], c1[4], gb[4], xv[4], cw[4];
  float scw, ssw;
  {
    const u32 mo = L_MR + (act ? lane * 32 : 0);
    float p0 = 0, p1 = 0;
#pragma unroll
    for (int k = 0; k < 4; k++) {
      ga[k] = *(LAS f32x2*)(L + mo + k * 8); sb[k] = *(LAS f32x2*)(L + mo + 1792 + k * 8);
      const f32x2 cb = *(LAS f32x2*)(L + mo + 3584 + k * 8); gb[k] = *(LAS f32x2*)(L + mo + 5376 + k * 8);
      const u32 t = pk(1.f + cb.x, 1.f + cb.y);
      c1[k] = (f32x2){lo(t), hi(t)};
      xv[k] = (f32x2){lo(xr[k]), hi(xr[k])};
      const f32x2 w = {lo(wrr[k]), hi(wrr[k])};
      cw[k] = c1[k] * w;
      if (!act) ga[k] = (f32x2){0, 0};
      p0 += cw[k].x + cw[k].y; p1 += sb[k].x * w.x + sb[k].y * w.y;
    }
    if (GSP == 2) { asm volatile("" : "+v"(p0), "+v"(p1)); gload(2); }
    if (CPD == 3) { asm volatile("" : "+v"(p0), "+v"(p1)); cfload(cf2, 2 * CRP); }
    scw = wsum(p0); ssw = wsum(p1);
#pragma unroll
    for (int k = 0; k < 4; k++) asm volatile("" : "+v"(ga[k]), "+v"(sb[k]), "+v"(c1[k]), "+v"(gb[k]), "+v"(xv[k]), "+v"(cw[k]));
    asm volatile("" : "+v"(scw), "+v"(ssw));
  }
  if (GSP == 0) { gload(0); gload(2); }
  u32 mb1 = mb_so + 7168, mb2 = mb_so + 14336, mb3 = mb_so + 21504;
  LAS char *y_w = L + L_Y + (lane & 3) * 2 * 1808 + (wave * 16 + (lane >> 2)) * 16, *y_r = L + L_Y + wave * 1808 + (act ? lane * 32 : 0);
  LAS char *rb_w = L + L_RB + wave * 8, *rb_r = L + L_RB + (lane & 7) * 8;
  asm volatile("" : "+s"(mb1), "+s"(mb2), "+s"(mb3)); asm volatile("" : "+v"(y_w), "+v"(y_r), "+v"(rb_w), "+v"(rb_r));
  if (CPD > 1) cfload(cf1, CRP);
  if (wave == 7) {
#define CFS(c, o) asm volatile("s_waitcnt vmcnt(%1)" : "+v"(c) : "n"(CPD - 1) : "memory"); if (clean<16>(bad(c, ep))) break; \
               asm volatile("buffer_load_dwordx2 %0, %1, %2, %3 offen sc1" : "+v"(c) : "v"(cf_vo), "s"(rws), "s"(o) : "memory");
    _Pragma("unroll 1") for (int it = 0; it < SPIN; it++) {
      CFS(cf, 0)
      if (CPD == 3) { CFS(cf2, 2 * CRP) }
      if (CPD > 1) { CFS(cf1, CRP) }
    }
  }
  barrier();
  TS(11);
  v4u p[4];
  asm volatile("s_cmp_eq_u32 %10, 7\n\ts_cbranch_scc1 1f\n\tbuffer_load_dwordx4 %0, %4, %5, %6 offen sc1\n\tbuffer_load_dwordx4 %1, %4, %5, %7 offen sc1\n\t"
    "buffer_load_dwordx4 %2, %4, %5, %8 offen sc1\n\tbuffer_load_dwordx4 %3, %4, %5, %9 offen sc1\n1:"
    : "=&v"(p[0]), "=&v"(p[1]), "=&v"(p[2]), "=&v"(p[3]) : "v"(lane16), "s"(rws), "s"(mb_so), "s"(mb1), "s"(mb2), "s"(mb3), "s"(wave) : "scc", "memory");
  if (wave < 7) {
    float y[8];
    asm volatile("s_waitcnt vmcnt(3)" : "+v"(p[0]) :: "memory"); acc8<1>(y, p[0]);
    asm volatile("s_waitcnt vmcnt(2)" : "+v"(p[1]) :: "memory"); acc8<0>(y, p[1]);
    asm volatile("s_waitcnt vmcnt(1)" : "+v"(p[2]) :: "memory"); acc8<0>(y, p[2]);
    asm volatile("s_waitcnt vmcnt(0)" : "+v"(p[3]) :: "memory"); acc8<0>(y, p[3]);
    *(LAS f32x4*)y_w = (f32x4){y[0], y[1], y[2], y[3]};
    asm volatile("s_nop 1" : "+v"(y[4]), "+v"(y[5]), "+v"(y[6]), "+v"(y[7]) :: "memory");
    *(LAS f32x4*)(y_w + 1808) = (f32x4){y[4], y[5], y[6], y[7]};
  } else {
    __builtin_amdgcn_s_setprio(0);
    if (r < 7) {
      if (USL) __builtin_amdgcn_s_sleep(USL);
#pragma unroll 1
      for (int w = 0; w < 7; w++)
#pragma unroll
        for (int i = 0; i < 4; i++) dma1k<0>(L_U + w * 4352 + i * 1088, mlp_vo, r_u, (w * 64 + i) * 3584 + mi * 64);
    }
  }
  WAITLGKM0(); barrier();
  TS(12);
  const rsrc_t rout = mk(out, S * D * 2);
  const int myrow = tq * 128 + o_self * 8 + wave;
  bool best;
  {
    const f32x4 ya = *(LAS f32x4*)y_r, yb = *(LAS f32x4*)(y_r + 16);
    f32x2 x1[4], d[4];
    x1[0] = ga[0] * (f32x2){ya[0], ya[1]} + xv[0]; x1[1] = ga[1] * (f32x2){ya[2], ya[3]} + xv[1];
    x1[2] = ga[2] * (f32x2){yb[0], yb[1]} + xv[2]; x1[3] = ga[3] * (f32x2){yb[2], yb[3]} + xv[3];
    const float piv = uf(__builtin_amdgcn_readfirstlane(fu(x1[0].x)));
    const float pv = act ? piv : 0.f;
#pragma unroll
    for (int k = 0; k < 4; k++) d[k] = x1[k] - pv;
    const f32x2 t1 = (d[0] + d[1]) + (d[2] + d[3]);
    const f32x2 t2 = d[0] * d[0] + d[1] * d[1] + d[2] * d[2] + d[3] * d[3];
    const f32x2 t3 = d[0] * cw[0] + d[1] * cw[1] + d[2] * cw[2] + d[3] * cw[3];
    float s1 = t1.x + t1.y, s2 = t2.x + t2.y, s3 = t3.x + t3.y;
    float w = sum16(sum32(s1, s2), sum32(s3, s3));
    w += dpp<QP1>(w); w += dpp<QP2>(w); w += dpp<ROR4>(w); w += dpp<ROR8>(w);
    const float m = uf(__builtin_amdgcn_readlane(fu(w), 0)) * (1.f / D);
    const float var = fmaxf(uf(__builtin_amdgcn_readlane(fu(w), 32)) * (1.f / D) - m * m, 0.f);
    const float rs = __builtin_amdgcn_rsqf(var + 1e-5f);
    const float rsc = rs * (uf(__builtin_amdgcn_readlane(fu(w), 16)) - m * scw) + ssw;
    const v2u key = {(u32)(7 - wave), fu(rsc) ^ ((u32)((int)fu(rsc) >> 31) | 0x80000000u)};
    if (lane == 0) *(LAS v2u*)rb_w = key;
    v4u dgv = {fu(rsc), (u32)(myrow + b * 65536), (u32)ep, (u32)(ep >> 32)};
    u32 dg_so = WS_DG + b * DGS, d3_vo = lane < DPD ? lane * DRP : OOB;
    u64 key0 = ((u64)(u32)__builtin_amdgcn_readfirstlane(key.y) << 32) | (u32)(7 - wave);
    asm volatile("" : "+s"(dg_so), "+s"(key0));
    if (CPD == 3) asm volatile("s_waitcnt vmcnt(0)" : "+v"(dgv), "+v"(d3_vo), "+v"(cf), "+v"(cf1), "+v"(cf2) :: "memory");
    else if (CPD == 2) asm volatile("s_waitcnt vmcnt(0)" : "+v"(dgv), "+v"(cf), "+v"(cf1) :: "memory");
    else asm volatile("s_waitcnt vmcnt(0)" : "+v"(dgv) :: "memory");
    WAITLGKM0(); barrier();
    best = __builtin_amdgcn_ballot_w64(*(LAS u64*)rb_r > key0) == 0;
    if (best) st16(dgv, rws, d3_vo, dg_so);
    u32 h2p[4]; f32x2 base[4];
#pragma unroll
    for (int k = 0; k < 4; k++) {
      const f32x2 h = ((d[k] - m) * rs) * c1[k] + sb[k];
      h2p[k] = pk(h.x, h.y);
      base[k] = gb[k] * (f32x2){lo(h2p[k]), hi(h2p[k])} + x1[k];
    }
    const v4u ov = {pk(base[0].x, base[0].y), pk(base[1].x, base[1].y), pk(base[2].x, base[2].y), pk(base[3].x, base[3].y)};
    if (best) {
      st16((v4u){h2p[0], h2p[1], (u32)ep, (u32)(ep >> 32)}, rws, l56_vo, WS_DH + b * 2048);
      st16((v4u){h2p[2], h2p[3], (u32)ep, (u32)(ep >> 32)}, rws, l56_vo, WS_DH + b * 2048 + 896);
#pragma unroll
      for (int k = 0; k < 4; k++) st16((v4u){fu(base[k].x), fu(base[k].y), (u32)ep, (u32)(ep >> 32)}, rws, l56_vo, WS_DB + b * 4096 + k * 896);
    }
    st16(ov, rout, l56_vo, myrow * 896);
    if (best && b >= NMLP) {
      WAITVM(0);
      st16((v4u){0, 0, (u32)ep, (u32)(ep >> 32)}, rws, l0_vo, WS_DF + b * 128);
    }
  }
  TS(13);
  if (b < NMLP) {
    v4u g = {0, 0, 0, 0};
    v16u da, db, dc;
    u32 sx = 0, h_so; float rwin = 0;
    LAS char* dp = L + L_D + wave * 4096 + (q * 8 + (l15 >> 2)) * 128 + (lane & 3) * 8;
    v4u ev = {0, 0, (u32)ep, (u32)(ep >> 32)};
    LAS char* gu_r = L + L_GU + ((l15 >> 3) * 32 + q * 8 + (lane & 7)) * 4;
    u32 e_vo = lane & 1 ? OOB : (wave * 32 + (lane >> 1)) * 16;
    if (wave == 7) {
      __builtin_amdgcn_s_setprio(3);
      u64 sa, sb, sc; u32 cnt, t, l, rw;
#define V4(R, i) "v[" #R "+" #i ":" #R "+" #i "+3]"
#define V2(R, i) "v[" #R "+" #i ":" #R "+" #i "+1]"
#define V1(R, i) "v[" #R "+" #i "]"
#define DV176 "%11"
#define DV192 "%22"
#define DV160 "%23"
#define DL1(R, i, o) "buffer_load_dwordx4 " V4(R, i) ", " DV##R ", %12, " o " offen sc1\n\t"
#define DLD(R) DL1(R, 0, "%14") DL1(R, 4, "%15") DL1(R, 8, "%16") DL1(R, 12, "%17")
#define DMX(c) "v_max_f32_dpp v213, v213, v213 " c " bank_mask:0xf\n\t"
#define STR(x) #x
#define XSTR(x) STR(x)
#define DSP
#define DLM(R)
#define DLU(R) DLD(R)
#define DSCAN(R) "s_waitcnt vmcnt(%21)\n\t" \
      "v_cmp_eq_u64 %4, %13, " V2(R, 2) "\n\tv_cmp_eq_u64 %5, %13, " V2(R, 6) "\n\tv_cmp_eq_u64 %6, %13, " V2(R, 10) "\n\tv_cmp_eq_u64 vcc, %13, " V2(R, 14) "\n\t" \
      DLM(R) \
      "v_cndmask_b32 v208, v214, " V1(R, 0) ", %4\n\tv_cndmask_b32 v209, v214, " V1(R, 4) ", %5\n\tv_cndmask_b32 v210, v214, " V1(R, 8) ", %6\n\tv_cndmask_b32 v211, v214, " V1(R, 12) ", vcc\n\t" \
      "s_and_b64 %4, %4, %5\n\tv_max3_f32 v212, v208, v209, v210\n\ts_and_b64 %6, %6, vcc\n\tv_max_f32 v212, v212, v211\n\ts_and_b64 %4, %4, %6\n\t" \
      "v_cmp_eq_f32 %5, v210, v212\n\tv_max_f32_dpp v213, v212, v212 quad_perm:[1,0,3,2] row_mask:0xf bank_mask:0xf\n\tv_cmp_eq_f32 %6, v209, v212\n\tv_cmp_eq_f32 vcc, v208, v212\n\t" \
      DMX("quad_perm:[2,3,0,1] row_mask:0xf") "v_cndmask_b32 v210, " V1(R, 13) ", " V1(R, 9) ", %5\n\tv_cndmask_b32 v210, v210, " V1(R, 5) ", %6\n\t" \
      DMX("row_ror:4 row_mask:0xf") "v_cndmask_b32 v210, v210, " V1(R, 1) ", vcc\n\ts_cmp_eq_u64 %4, -1\n\t" \
      DMX("row_ror:8 row_mask:0xf") "s_cselect_b32 %8, 0x81, 1\n\ts_lshl_b32 %8, %8, 24\n\t" \
      DMX("row_bcast:15 row_mask:0xa") "s_nop 1\n\t" DMX("row_bcast:31 row_mask:0xc") "s_nop 0\n\tv_readlane_b32 %3, v213, 63\n\ts_nop 1\n\t" \
      "v_cmp_eq_f32 vcc, %3, v212\n\ts_ff1_i32_b64 %9, vcc\n\ts_max_i32 %9, %9, 0\n\tv_readlane_b32 %2, v210, %9\n\ts_and_b32 %2, %2, 0xffffff\n\ts_or_b32 %2, %2, %8\n\t" \
      "v_mov_b32 v208, %2\n\tds_write_b32 v215, v208\n\ts_cmp_lt_i32 %2, 0\n\ts_cbranch_scc1 2f\n\t" DLU(R) DSP
      asm volatile(DLD(176) "v_mov_b32 v214, 0xff800000\n\tv_mov_b32 v215, %18\n\ts_mov_b32 %7, %19\n\ts_sleep %20\n\t" DLD(192)
        "1:\n\t" DSCAN(176) DSCAN(192)
        "s_sub_u32 %7, %7, 1\n\ts_cbranch_scc0 1b\n2:"
        : "=&{v[176:191]}"(da), "=&{v[192:207]}"(db), "=&s"(sx), "=&s"(rw), "=&s"(sa), "=&s"(sb), "=&s"(sc), "=&s"(cnt), "=&s"(t), "=&s"(l), "=&{v[160:175]}"(dc)
        : "v"(lane * DGS), "s"(rws), "s"(ep), "s"(WS_DG), "s"(WS_DG + 64 * DGS), "s"(WS_DG + 128 * DGS), "s"(WS_DG + 192 * DGS), "n"(L_CNT + 48), "n"(SPIN), "n"(DSL), "n"(DPD * 4 - 4), "v"(lane * DGS + DRP), "v"(lane * DGS + 2 * DRP)
        : "vcc", "scc", "memory", "v208", "v209", "v210", "v211", "v212", "v213", "v214", "v215");
      sx = __builtin_amdgcn_readfirstlane(sx); rwin = uf(rw);
      h_so = WS_DB + ((sx >> 16) & 255) * 4096 + mi * 16;
      ld16r<1>(g, rws, (lane & 3) * 896, h_so);
      if (best) st16(ev, rws, l0_vo, WS_DF + b * 128);
      TS(14);
    } else {
      const u32 h_vo = (lane & 1) * 896 + (l15 >> 1) * 16;
#pragma unroll
      for (int i = 0; i < 4; i++) dma1k<0>(L_D + wave * 4096 + i * 1024, (lane >> 3) * 896 + (lane & 7) * 16, r_dn, (mi * 32 + i * 8) * 896 + wave * 128);
      asm volatile("" : "={v[176:191]}"(da), "={v[192:207]}"(db), "={v[160:175]}"(dc));
      v2u gw[16];
      LAS char* gp = L + (lane < 32 ? L_G : L_U) + wave * 4352 + (l15 >> 2) * 1088 + (q & 1) * 32 + (lane & 3) * 8;
#pragma unroll
      for (int i = 0; i < 16; i++) gw[i] = ldtr(gp + i * 64);
      float a0 = 0, a1 = 0, a2 = 0, a3 = 0;
      LAS char *gu_w = L + L_GU + wave * 256 + lane * 4;
      asm volatile("" : "+v"(gw[0]), "+v"(gw[1]), "+v"(gw[2]), "+v"(gw[3]), "+v"(gw[4]), "+v"(gw[5]), "+v"(gw[6]), "+v"(gw[7]),
                        "+v"(gw[8]), "+v"(gw[9]), "+v"(gw[10]), "+v"(gw[11]), "+v"(gw[12]), "+v"(gw[13]), "+v"(gw[14]), "+v"(gw[15]));
      asm volatile("" : "+v"(a0), "+v"(a1), "+v"(a2), "+v"(a3), "+v"(dp), "+v"(gu_w), "+v"(gu_r), "+v"(e_vo), "+v"(ev));
      u32 sp, cnt, t;
      asm volatile("s_mov_b32 %2, 0\n\ts_mov_b32 %3, 0x400000\n"
        "1:\n\tds_read_b32 %5, %6\n\ts_waitcnt lgkmcnt(0)\n\tv_readfirstlane_b32 %1, %5\n\ts_xor_b32 %2, %1, %2\n\ts_and_b32 %2, %2, 0x1ff0000\n\ts_cbranch_scc0 2f\n\t"
        "s_bfe_u32 %4, %1, 0x80010\n\ts_lshl_b32 %4, %4, 11\n\ts_add_u32 %4, %4, %9\n\tbuffer_load_dwordx4 %0, %7, %8, %4 offen sc1\n"
        "2:\n\ts_mov_b32 %2, %1\n\ts_cmp_lt_i32 %1, 0\n\ts_cbranch_scc1 3f\n\ts_sub_u32 %3, %3, 1\n\ts_cbranch_scc0 1b\n3:"
        : "+v"(g), "=&s"(sx), "=&s"(sp), "=&s"(cnt), "=&s"(h_so), "=&v"(t) : "v"(L_CNT + 48), "v"(h_vo), "s"(rws), "s"(WS_DH + wave * 128) : "scc", "memory");
      TS(14);
      asm volatile("s_waitcnt vmcnt(0)" : "+v"(g) :: "memory");
      TY(25);
      if (!clean<64>(bad(g, ep))) {
        h_so = __builtin_amdgcn_readfirstlane(h_so);
        _Pragma("unroll 1") for (int it = 0; it < SPIN; it++) {
          ld16r<1>(g, rws, h_vo, h_so);
          asm volatile("s_waitcnt vmcnt(0)" : "+v"(g) :: "memory");
          if (clean<64>(bad(g, ep))) break;
        }
      }
      dotb8<0>(a0, a1, g.x, g.y, gw); dotb8<8>(a2, a3, g.x, g.y, gw + 8);
      asm volatile("v_add_f32 %0, %0, %1\n\ts_nop 1\n\tv_add_f32 %2, %2, %3\n\tv_add_f32 %0, %0, %2" : "+v"(a0), "+v"(a1), "+v"(a2), "+v"(a3));
      *(LAS float*)gu_w = a0;
      if (best) st16(ev, rws, l0_vo, WS_DF + b * 128);
    }
    WAITLGKM0(); barrier();
    TY(26);
    if (wave < 7) {
      float u[7];
#pragma unroll
      for (int k = 0; k < 7; k++) u[k] = *(LAS float*)(gu_r + k * 256);
      SCHED();
      v4u dw[4];
#pragma unroll
      for (int t = 0; t < 4; t++) { const v2u u0 = ldtr(dp + t * 32), u1 = ldtr(dp + t * 32 + 512); dw[t] = (v4u){u0.x, u0.y, u1.x, u1.y}; }
      SCHED();
      const float v = rbf(((u[0] + u[1]) + (u[2] + u[3])) + ((u[4] + u[5]) + u[6]));
      const float av = rbf(v * sigm(v)) * dpp<ROR8>(v);
      const u32 ap = pk(av, dpp<QP1>(av));
      const v4u am = {fu(dpp<0x150>(uf(ap))), fu(dpp<0x152>(uf(ap))), fu(dpp<0x154>(uf(ap))), fu(dpp<0x156>(uf(ap)))};
      f32x4 y[4];
#pragma unroll
      for (int t = 0; t < 4; t++) y[t] = mfma(am, dw[t], (f32x4){0, 0, 0, 0});
      u32 o = fu(y[0][0]);
      o = __builtin_amdgcn_update_dpp(o, fu(y[1][0]), 0xe4, 0x2, 0xf, false);
      o = __builtin_amdgcn_update_dpp(o, fu(y[2][0]), 0xe4, 0x4, 0xf, false);
      o = __builtin_amdgcn_update_dpp(o, fu(y[3][0]), 0xe4, 0x8, 0xf, false);
      ev.x = o; ev.y = fu(dpp<QP1>(uf(o)));
      st16(ev, rws, e_vo, WS_EG + mi * 3584);
      TY(27);
    }
    if (wave == 7) {
      const int win = (sx >> 16) & 255;
      u32 o_so = __builtin_amdgcn_readfirstlane((sx & 2047) * 896 + mi * 16), o_vo = (lane & 44) == 0 ? (lane & 3) * 4 + (lane >> 4) * 2 : OOB;
      asm volatile("" : "+s"(o_so)); asm volatile("" : "+v"(o_vo));
      const u32 vo = (lane >> 2) * 3584 + (lane & 3) * 16, vo3 = lane < 32 ? vo + WS_EG + 48 * 3584 + mi * 64 : WS_DF + win * 128;
      float gs = *(LAS float*)(L + L_MR + 5376 + mi * 32 + (lane & 3) * 8 + (q & 1) * 4) * sigm(rwin), y0, y1, t0, t1;
      asm volatile("s_waitcnt vmcnt(0)" : "+v"(g), "+{v[176:191]}"(da), "+{v[192:207]}"(db), "+{v[160:175]}"(dc) :: "memory");
      if (!clean<64>(bad(g, ep))) {
        _Pragma("unroll 1") for (int it = 0; it < SPIN; it++) {
          ld16r<1>(g, rws, (lane & 3) * 896, __builtin_amdgcn_readfirstlane(WS_DB + win * 4096 + mi * 16));
          asm volatile("s_waitcnt vmcnt(0)" : "+v"(g) :: "memory");
          if (clean<64>(bad(g, ep))) break;
        }
      }
      float bs = uf(q & 1 ? g.y : g.x);
      asm volatile("" : "+v"(gs), "+v"(bs));
      u64 sa, sb, sc, sd; u32 cnt;
#define EL1(R, i, v, o) "buffer_load_dwordx4 " V4(R, i) ", " v ", %15, " o " offen sc1\n\t"
#define ELD(R) EL1(R, 0, "%13", "%17") EL1(R, 4, "%13", "%18") EL1(R, 8, "%13", "%19") EL1(R, 12, "%14", "0")
#define ELM(R) ELD(R)
#define ECHK(R, l) "s_waitcnt vmcnt(%22)\n\tv_cmp_ne_u64 %8, %16, " V2(R, 2) "\n\tv_cmp_ne_u64 %9, %16, " V2(R, 6) "\n\tv_cmp_ne_u64 %10, %16, " V2(R, 10) "\n\tv_cmp_ne_u64 vcc, %16, " V2(R, 14) "\n\t" \
      "s_or_b64 %12, %8, %9\n\ts_or_b64 %12, %12, %10\n\ts_or_b64 %12, %12, vcc\n\ts_cbranch_scc0 " l "\n\t" ELM(R)
#define ESUM(R) "v_add_f32 %4, " V1(R, 0) ", " V1(R, 4) "\n\tv_add_f32 %6, " V1(R, 8) ", " V1(R, 12) "\n\tv_add_f32 %5, " V1(R, 1) ", " V1(R, 5) "\n\tv_add_f32 %7, " V1(R, 9) ", " V1(R, 13) "\n\t"
      if (EIN) __builtin_amdgcn_s_sleep(EIN);
      asm volatile(ELD(176) "s_mov_b32 %11, %20\n\ts_sleep %21\n\t" ELD(192)
        "s_sleep %21\n\t" ELD(160)
        "1:\n\t" ECHK(176, "2f") ECHK(192, "3f")
        ECHK(160, "4f")
        "s_sub_u32 %11, %11, 1\n\ts_cbranch_scc0 1b\n"
        "2:\n\t" ESUM(176) "s_branch 5f\n3:\n\t" ESUM(192) "s_branch 5f\n4:\n\t" ESUM(160) "\n5:\n\tv_add_f32 %4, %4, %6\n\tv_add_f32 %5, %5, %7\n\ts_nop 1"
        : "+{v[176:191]}"(da), "+{v[192:207]}"(db), "+{v[160:175]}"(dc), "+v"(g), "=&v"(y0), "=&v"(y1), "=&v"(t0), "=&v"(t1), "=&s"(sa), "=&s"(sb), "=&s"(sc), "=&s"(cnt), "=&s"(sd)
        : "v"(vo), "v"(vo3), "s"(rws), "s"(ep), "s"(WS_EG + mi * 64), "s"(WS_EG + 16 * 3584 + mi * 64), "s"(WS_EG + 32 * 3584 + mi * 64), "n"(SPIN), "n"(ESL), "n"(EPD * 4 - 4)
        : "vcc", "scc", "memory");
      y0 = sum16(y0, y1); y0 = sum32(y0, y0); y0 += dpp<ROR4>(y0); y0 += dpp<ROR8>(y0);
      __builtin_amdgcn_raw_buffer_store_b16((unsigned short)pk(fmaf(gs, y0, bs), 0.f), rout, o_vo, o_so, FINA);
      if (b == 0) __builtin_amdgcn_raw_buffer_store_b64(epv2, rws, l0_vo, 0, FINA);
      asm volatile("" :: "{v[176:191]}"(da), "{v[192:207]}"(db), "{v[160:175]}"(dc) : "memory");
    }
  }
  asm volatile(".Lfk_end:");
}
}

extern "C" void kernel_launch(void* const* d_in, const int* in_sizes, int n_in, void* d_out, int out_size, void* d_ws, size_t ws_size,
                              hipStream_t stream) {
  (void)hipFuncSetAttribute((const void*)fused_kernel, hipFuncAttributeMaxDynamicSharedMemorySize, L_SIZE);
  fused_kernel<<<256, 512, L_SIZE, stream>>>((const char*)d_in[0], (const char*)d_in[1], (const char*)d_in[2], (const char*)d_in[3],
      (const char*)d_in[4], (const char*)d_in[6], (const char*)d_in[7], (const char*)d_in[8], (const char*)d_in[9], (const char*)d_in[10],
      (const char*)d_in[11], (char*)d_out, (char*)d_ws);
}
```
